```python
import torch

def _softmax(x, axis=-1):
    return torch.softmax(x, dim=axis)
def _gelu(x):
    return torch.nn.functional.gelu(x, approximate="tanh")
def _silu(x):
    return torch.nn.functional.silu(x)
def _sigmoid(x):
    return torch.sigmoid(x)
def _layernorm(x, axis=-1, eps=1e-5):
    m = x.mean(dim=axis, keepdim=True)
    v = x.var(dim=axis, keepdim=True, unbiased=False)
    return (x - m) / torch.sqrt(v + eps)
def _scatter_add(tgt, idx, src, axis=0):
    return tgt.clone().index_add_(axis, idx, src)
def _segment_sum(src, seg, n):
    out = torch.zeros((n,) + src.shape[1:], dtype=src.dtype)
    return out.index_add_(0, seg, src)
def _pad(x, pw):
    flat = tuple(v for pair in reversed(pw) for v in pair)
    return torch.nn.functional.pad(x, flat)

kernel_name = 'standalone__glu_gated_swiglu__prompt'

def setup_inputs():

    _w = (lambda a: (lambda t: t.to(torch.bfloat16) if t.is_floating_point() else t)(torch.as_tensor(a)))
    B, S, D, F = 1, 2048, 512, 1376
    return {
        "x": _w((lambda *s: torch.randn(*s, dtype=torch.bfloat16) * 0.05)(B, S, D)),
        "w1": _w((lambda *s: torch.randn(*s, dtype=torch.bfloat16) * 0.05)(D, F) * (2.0 / D) ** 0.5),
        "w3": _w((lambda *s: torch.randn(*s, dtype=torch.bfloat16) * 0.05)(D, F) * (2.0 / D) ** 0.5),
        "w2": _w((lambda *s: torch.randn(*s, dtype=torch.bfloat16) * 0.05)(F, D) * (2.0 / F) ** 0.5),
    }

def reference(x, w1, w3, w2):
    gate = torch.einsum("bsd,df->bsf", x, w1)
    up = torch.einsum("bsd,df->bsf", x, w3)
    h = _silu(gate) * up
    out = torch.einsum("bsf,fd->bsd", h, w2)
    return out
```

```cpp
#include <hip/hip_runtime.h>
#include <hip/hip_bf16.h>
typedef __attribute__((ext_vector_type(8))) short bf16x8;
typedef __attribute__((ext_vector_type(4))) float f32x4;
constexpr int S=2048, D=512, F=1376;
__device__ __forceinline__ unsigned short f2bf(float x){ __hip_bfloat16 b = __float2bfloat16(x); return *(unsigned short*)&b; }
__device__ __forceinline__ float rbf(float x){ return __bfloat162float(__float2bfloat16(x)); }
__global__ void k1(const unsigned short* x, const unsigned short* w1, const unsigned short* w3, unsigned short* h){
  int wave = blockIdx.x*4 + (threadIdx.x>>6); int lane = threadIdx.x&63;
  int mt = wave / (F/16), ft = wave % (F/16);
  if (mt >= S/16) return;
  f32x4 g={0,0,0,0}, u={0,0,0,0};
  int r = lane&15, q = lane>>4;
  for(int k=0;k<D;k+=32){
    bf16x8 a = *(const bf16x8*)(x + (size_t)(mt*16+r)*D + k + q*8);
    bf16x8 b1,b3;
    for(int j=0;j<8;j++){ b1[j]=w1[(size_t)(k+q*8+j)*F + ft*16+r]; b3[j]=w3[(size_t)(k+q*8+j)*F + ft*16+r]; }
    g = __builtin_amdgcn_mfma_f32_16x16x32_bf16(a,b1,g,0,0,0);
    u = __builtin_amdgcn_mfma_f32_16x16x32_bf16(a,b3,u,0,0,0);
  }
  for(int j=0;j<4;j++){
    float gg=g[j], uu=u[j];
    float s = gg/(1.f+__expf(-gg));
    h[(size_t)(mt*16+q*4+j)*F + ft*16+r] = f2bf(s*uu);
  }
}
__global__ void k2(const unsigned short* h, const unsigned short* w2, unsigned short* out){
  int wave = blockIdx.x*4 + (threadIdx.x>>6); int lane = threadIdx.x&63;
  int mt = wave / (D/16), nt = wave % (D/16);
  f32x4 g={0,0,0,0};
  int r = lane&15, q = lane>>4;
  for(int k=0;k<F;k+=32){
    bf16x8 a = *(const bf16x8*)(h + (size_t)(mt*16+r)*F + k + q*8);
    bf16x8 b1;
    for(int j=0;j<8;j++){ b1[j]=w2[(size_t)(k+q*8+j)*D + nt*16+r]; }
    g = __builtin_amdgcn_mfma_f32_16x16x32_bf16(a,b1,g,0,0,0);
  }
  for(int j=0;j<4;j++) out[(size_t)(mt*16+q*4+j)*D + nt*16+r] = f2bf(g[j]);
}
extern "C" void kernel_launch(void* const* d_in, const int* in_sizes, int n_in, void* d_out, int out_size, void* d_ws, size_t ws_size, hipStream_t stream) {
  const unsigned short* x=(const unsigned short*)d_in[0]; const unsigned short* w1=(const unsigned short*)d_in[1];
  const unsigned short* w3=(const unsigned short*)d_in[2]; const unsigned short* w2=(const unsigned short*)d_in[3];
  unsigned short* h=(unsigned short*)d_ws;
  k1<<<(S/16)*(F/16)/4,256,0,stream>>>(x,w1,w3,h);
  k2<<<(S/16)*(D/16)/4,256,0,stream>>>(h,w2,(unsigned short*)d_out);
}
```

```cpp
#include <hip/hip_runtime.h>

__global__ __attribute__((aligned(4096))) __launch_bounds__(256, 1) void swiglu_fused(const void* x, const void* w1, const void* w3, const void* w2, void* out, void* ws) {
#if defined(__HIP_DEVICE_COMPILE__)
  __shared__ __attribute__((aligned(1024))) char lds[163840];
  asm volatile(
    "s_load_dwordx2 s[4:5], %0, 0x0\n\t"
    "v_lshrrev_b32 v28, 6, %2\n\t"
    "v_and_b32 v1, 63, %2\n\t"
    "v_readfirstlane_b32 s30, v28\n\t"
    "s_load_dwordx2 s[16:17], %0, 0x28\n\t"
    "s_getpc_b64 s[80:81]\n\t"
    "s_and_b32 s82, %1, 7\n\t"
    "s_lshr_b32 s83, %1, 3\n\t"
    "s_and_b32 s78, s30, 1\n\t"
    "s_lshl3_add_u32 s47, s78, 8\n\t"
    "s_load_dwordx2 s[8:9], %0, s47\n\t"
    "v_lshl_add_u32 v28, s83, 2, v28\n\t"
    "v_lshlrev_b32 v28, 7, v28\n\t"
    "v_min_u32 v28, 15360, v28\n\t"
    "global_load_dword v13, v28, s[80:81]\n\t"
    "s_load_dwordx2 s[12:13], %0, 0x18\n\t"
    "s_load_dwordx2 s[20:21], %0, 0x20\n\t"
    "s_lshr_b32 s31, s30, 1\n\t"
    "s_lshr_b32 s47, s83, 2\n\t"
    "s_and_b32 s48, s82, 3\n\t"
    "s_and_b32 s49, s83, 3\n\t"
    "s_and_b32 s79, s82, 4\n\t"
    "s_lshl1_add_u32 s79, s79, s47\n\t"
    "s_add_u32 s86, s47, s49\n\t"
    "s_lshl2_add_u32 s86, s86, s48\n\t"
    "s_lshr_b32 s50, s82, 2\n\t"
    "s_lshl1_add_u32 s86, s50, s86\n\t"
    "s_and_b32 s86, s86, 7\n\t"
    "s_mov_b32 s6, 2097152\n\t"
    "s_mov_b32 s7, 0x20000\n\t"
    "s_mov_b32 s10, 1411072\n\t"
    "s_mov_b32 s11, 0x20000\n\t"
    "s_mov_b32 s18, 16777216\n\t"
    "s_mov_b32 s19, 0x20000\n\t"
    "s_mov_b32 s14, 1409024\n\t"
    "s_mov_b32 s15, 0x20000\n\t"
    "v_lshrrev_b32 v28, 3, v1\n\t"
    "v_lshrrev_b32 v29, 4, v1\n\t"
    "v_and_b32 v30, 7, v1\n\t"
    "s_lshl2_add_u32 s34, s48, s49\n\t"
    "s_mul_i32 s36, s48, 11\n\t"
    "s_mul_i32 s35, s49, 3\n\t"
    "s_add_u32 s35, s35, s36\n\t"
    "s_add_u32 s36, s36, 11\n\t"
    "s_min_u32 s36, s36, 43\n\t"
    "s_sub_u32 s36, s36, s35\n\t"
    "s_lshl2_add_u32 s39, s79, s30\n\t"
    "s_lshl_b32 s39, s39, 15\n\t"
    "s_mul_i32 s40, s31, 44032\n\t"
    "s_lshl_b32 s42, s30, 12\n\t"
    "s_mul_i32 s43, s30, 3072\n\t"
    "s_add_u32 s43, s43, 65536\n\t"
    "v_lshlrev_b32 v32, 10, v28\n\t"
    "v_xor_b32 v31, v30, v29\n\t"
    "v_lshl_add_u32 v2, v31, 4, v32\n\t"
    "v_xor_b32 v3, 64, v2\n\t"
    "v_add_u32 v4, 14336, v2\n\t"
    "v_add_u32 v5, 21504, v3\n\t"
    "v_add_u32 v3, 7168, v3\n\t"
    "s_lshl_b32 s44, s86, 7\n\t"
    "s_add_u32 s44, s44, s39\n\t"
    "s_mul_i32 s45, s86, 176128\n\t"
    "s_add_u32 s45, s45, s40\n\t"
    "s_add_u32 s46, s45, 88064\n\t"
    "s_add_u32 m0, s42, 0\n\t"
    "v_lshrrev_b32 v28, 2, v1\n\t"
    "v_mul_u32_u24 v28, 2752, v28\n\t"
    "v_and_b32 v31, 3, v1\n\t"
    "v_and_b32 v32, 2, v29\n\t"
    "v_xor_b32 v31, v32, v31\n\t"
    "v_lshl_add_u32 v28, v31, 4, v28\n\t"
    "s_lshl_b32 s50, s35, 6\n\t"
    "s_add_u32 s51, s50, 2048\n\t"
    "v_add_u32 v6, s51, v28\n\t"
    "s_add_u32 s51, s50, 1088\n\t"
    "s_cmp_gt_u32 s36, 1\n\t"
    "s_cselect_b32 s51, s51, 1073741824\n\t"
    "v_add_u32 v7, s51, v28\n\t"
    "s_add_u32 s51, s50, 128\n\t"
    "s_cmp_gt_u32 s36, 2\n\t"
    "s_cselect_b32 s51, s51, 1073741824\n\t"
    "v_add_u32 v8, s51, v28\n\t"
    "s_waitcnt lgkmcnt(0)\n\t"
    "buffer_load_dwordx4 v2, s[4:7], s44 offen offset:0 lds\n\t"
    "buffer_load_dwordx4 v3, s[4:7], s44 offen offset:1024 lds\n\t"
    "buffer_load_dwordx4 v4, s[4:7], s44 offen offset:2048 lds\n\t"
    "buffer_load_dwordx4 v5, s[4:7], s44 offen offset:3072 lds\n\t"
    "s_sub_u32 s8, s8, 2048\n\t"
    "s_subb_u32 s9, s9, 0\n\t"
    "s_add_u32 m0, s43, 0\n\t"
    "s_nop 0\n\t"
    "buffer_load_dwordx4 v6, s[8:11], s45 offen offset:0 lds\n\t"
    "buffer_load_dwordx4 v7, s[8:11], s45 offen offset:1024 lds\n\t"
    "buffer_load_dwordx4 v8, s[8:11], s45 offen offset:2048 lds\n\t"
    "s_lshr_b32 s52, s83, 3\n\t"
    "s_lshl2_add_u32 s37, s82, s52\n\t"
    "s_and_b32 s38, s83, 7\n\t"
    "s_lshl1_add_u32 s51, s79, s31\n\t"
    "s_lshl_b32 s51, s51, 6\n\t"
    "s_add_u32 s51, s51, 6422528\n\t"
    "v_lshl_add_u32 v23, v30, 3, s51\n\t"
    "s_lshl3_add_u32 s93, s37, s38\n\t"
    "s_lshl3_add_u32 s51, s93, 6422528\n\t"
    "s_mov_b32 exec_hi, 0\n\t"
    "v_mov_b32 v23, s51\n\t"
    "s_mov_b32 exec_hi, -1\n\t"
    "buffer_load_dwordx2 v[24:25], v23, s[16:19], 0 offen sc1\n\t"
    "s_lshl2_add_u32 s87, s93, s30\n\t"
    "s_lshl_b32 s87, s87, 7\n\t"
    "s_add_u32 s87, s87, 6291456\n\t"
    "v_mov_b32 v26, 0\n\t"
    "v_lshlrev_b32 v27, 6, v1\n\t"
    "s_mov_b64 exec, 3\n\t"
    "buffer_atomic_add v26, v27, s[16:19], s87 offen\n\t"
    "s_mov_b64 exec, -1\n\t"
    "s_add_u32 m0, s43, 12288\n\t"
    "s_nop 0\n\t"
    "buffer_load_dwordx4 v6, s[8:11], s46 offen offset:0 lds\n\t"
    "buffer_load_dwordx4 v7, s[8:11], s46 offen offset:1024 lds\n\t"
    "buffer_load_dwordx4 v8, s[8:11], s46 offen offset:2048 lds\n\t"
    "v_lshrrev_b32 v33, 3, v1\n\t"
    "v_lshl_add_u32 v33, v33, 2, s86\n\t"
    "v_add_u32 v33, 1, v33\n\t"
    "v_and_b32 v33, 7, v33\n\t"
    "v_mul_u32_u24 v33, 176128, v33\n\t"
    "v_lshl_add_u32 v33, v30, 7, v33\n\t"
    "s_lshl1_add_u32 s87, s83, s31\n\t"
    "s_mul_i32 s87, s87, 2752\n\t"
    "s_mul_i32 s88, s48, 704\n\t"
    "s_add_u32 s87, s87, s88\n\t"
    "s_add_u32 s87, s87, 2048\n\t"
    "s_mov_b64 exec, 0x3f3f\n\t"
    "buffer_load_dword v14, v33, s[8:11], s87 offen\n\t"
    "s_mov_b64 exec, -1\n\t"
    "s_add_u32 s47, s47, s49\n\t"
    "s_mov_b32 s41, 0x73625140\n\t"
    "s_mov_b32 s50, 0x76352140\n\t"
    "s_bitcmp1_b32 s47, 0\n\t"
    "s_cselect_b32 s41, 0x73625140, s41\n\t"
    "s_cselect_b32 s50, 0x76352140, s50\n\t"
    "s_bitcmp1_b32 s47, 1\n\t"
    "s_cselect_b32 s41, s50, s41\n\t"
    "s_mul_i32 s47, s86, 0x11111111\n\t"
    "s_add_u32 s41, s41, s47\n\t"
    "v_and_b32 v28, 15, v1\n\t"
    "s_lshl_b32 s47, s31, 13\n\t"
    "v_lshl_add_u32 v28, v28, 7, s47\n\t"
    "v_bfe_u32 v30, v1, 1, 3\n\t"
    "v_xor_b32 v30, v30, v29\n\t"
    "v_lshl_add_u32 v9, v30, 4, v28\n\t"
    "v_xor_b32 v10, 64, v9\n\t"
    "v_mul_u32_u24 v30, 3072, v32\n\t"
    "v_bfe_u32 v28, v1, 4, 1\n\t"
    "v_lshl_add_u32 v30, v28, 9, v30\n\t"
    "v_xor_b32 v28, s78, v28\n\t"
    "v_lshl_add_u32 v30, v28, 5, v30\n\t"
    "v_bfe_u32 v28, v1, 2, 2\n\t"
    "v_lshl_add_u32 v30, v28, 6, v30\n\t"
    "v_and_b32 v28, 3, v1\n\t"
    "v_lshl_add_u32 v30, v28, 3, v30\n\t"
    "v_add_u32 v11, 65536, v30\n\t"
    "v_add_u32 v12, 114688, v30\n\t"
    "s_bfe_u32 s86, s41, 196612\n\t"
    "s_lshl_b32 s44, s86, 7\n\t"
    "s_add_u32 s44, s44, s39\n\t"
    "s_mul_i32 s45, s86, 176128\n\t"
    "s_add_u32 s45, s45, s40\n\t"
    "s_add_u32 s46, s45, 88064\n\t"
    "s_mov_b64 vcc, -1\n\t"
    "s_cmp_eq_u32 s30, 1\n\t"
    "s_cbranch_scc1 L_hop1_%=\n\t"
    "s_cmp_eq_u32 s30, 2\n\t"
    "s_cbranch_scc1 L_hop2_%=\n\t"
    "s_cmp_eq_u32 s30, 3\n\t"
    "s_cbranch_scc1 L_hop3_%=\n\t"
    "L_hopback%=:\n\t"
    "s_mov_b64 vcc, 0\n\t"
    "s_add_u32 m0, s42, 16384\n\t"
    "s_nop 0\n\t"
    "buffer_load_dwordx4 v2, s[4:7], s44 offen offset:0 lds\n\t"
    "buffer_load_dwordx4 v3, s[4:7], s44 offen offset:1024 lds\n\t"
    "buffer_load_dwordx4 v4, s[4:7], s44 offen offset:2048 lds\n\t"
    "buffer_load_dwordx4 v5, s[4:7], s44 offen offset:3072 lds\n\t"
    "s_add_u32 m0, s43, 24576\n\t"
    "s_nop 0\n\t"
    "buffer_load_dwordx4 v6, s[8:11], s45 offen offset:0 lds\n\t"
    "buffer_load_dwordx4 v7, s[8:11], s45 offen offset:1024 lds\n\t"
    "buffer_load_dwordx4 v8, s[8:11], s45 offen offset:2048 lds\n\t"
    "s_add_u32 m0, s43, 36864\n\t"
    "s_nop 0\n\t"
    "buffer_load_dwordx4 v6, s[8:11], s46 offen offset:0 lds\n\t"
    "buffer_load_dwordx4 v7, s[8:11], s46 offen offset:1024 lds\n\t"
    "buffer_load_dwordx4 v8, s[8:11], s46 offen offset:2048 lds\n\t"
    "s_bfe_u32 s86, s41, 196616\n\t"
    "s_lshl_b32 s44, s86, 7\n\t"
    "s_add_u32 s44, s44, s39\n\t"
    "s_mul_i32 s45, s86, 176128\n\t"
    "s_add_u32 s45, s45, s40\n\t"
    "s_add_u32 s46, s45, 88064\n\t"
    "s_waitcnt vmcnt(14)\n\t"
    "s_barrier\n\t"
    "ds_read_b128 v[104:107], v9 offset:0\n\t"
    "ds_read_b64_tr_b16 v[80:81], v11 offset:0\n\t"
    "ds_read_b64_tr_b16 v[82:83], v11 offset:256\n\t"
    "ds_read_b128 v[108:111], v9 offset:2048\n\t"
    "ds_read_b128 v[112:115], v9 offset:4096\n\t"
    "ds_read_b128 v[116:119], v9 offset:6144\n\t"
    "ds_read_b64_tr_b16 v[84:85], v11 offset:3072\n\t"
    "ds_read_b64_tr_b16 v[86:87], v11 offset:3328\n\t"
    "ds_read_b64_tr_b16 v[88:89], v11 offset:1024\n\t"
    "ds_read_b64_tr_b16 v[90:91], v11 offset:1280\n\t"
    "ds_read_b64_tr_b16 v[92:93], v11 offset:4096\n\t"
    "ds_read_b64_tr_b16 v[94:95], v11 offset:4352\n\t"
    "s_waitcnt lgkmcnt(6)\n\t"
    "v_mfma_f32_16x16x32_bf16 v[160:163], v[80:83], v[104:107], 0\n\t"
    "s_add_u32 m0, s42, 32768\n\t"
    "ds_read_b64_tr_b16 v[96:97], v11 offset:2048\n\t"
    "v_mfma_f32_16x16x32_bf16 v[164:167], v[80:83], v[108:111], 0\n\t"
    "buffer_load_dwordx4 v2, s[4:7], s44 offen offset:0 lds\n\t"
    "ds_read_b64_tr_b16 v[98:99], v11 offset:2304\n\t"
    "v_mfma_f32_16x16x32_bf16 v[168:171], v[80:83], v[112:115], 0\n\t"
    "ds_read_b64_tr_b16 v[100:101], v11 offset:5120\n\t"
    "v_mfma_f32_16x16x32_bf16 v[172:175], v[80:83], v[116:119], 0\n\t"
    "s_waitcnt lgkmcnt(7)\n\t"
    "ds_read_b64_tr_b16 v[102:103], v11 offset:5376\n\t"
    "v_mfma_f32_16x16x32_bf16 v[176:179], v[84:87], v[104:107], 0\n\t"
    "s_lshl2_add_u32 s51, %1, s30\n\t"
    "v_lshl_add_u64 v[24:25], v[24:25], 0, 1\n\t"
    "v_mfma_f32_16x16x32_bf16 v[180:183], v[84:87], v[108:111], 0\n\t"
    "s_mul_i32 s51, s51, 1408\n\t"
    "buffer_load_dwordx4 v3, s[4:7], s44 offen offset:1024 lds\n\t"
    "v_mfma_f32_16x16x32_bf16 v[184:187], v[84:87], v[112:115], 0\n\t"
    "s_mov_b32 s22, 2097152\n\t"
    "v_lshlrev_b32 v33, 7, v1\n\t"
    "v_mfma_f32_16x16x32_bf16 v[188:191], v[84:87], v[116:119], 0\n\t"
    "s_waitcnt lgkmcnt(4)\n\t"
    "v_add_u32 v34, 53, v1\n\t"
    "v_mfma_f32_16x16x32_bf16 v[192:195], v[88:91], v[104:107], 0\n\t"
    "s_mov_b32 s23, 0x20000\n\t"
    "v_and_b32 v34, 64, v34\n\t"
    "v_mfma_f32_16x16x32_bf16 v[196:199], v[88:91], v[108:111], 0\n\t"
    "s_mov_b32 s26, 44032\n\t"
    "buffer_load_dwordx4 v4, s[4:7], s44 offen offset:2048 lds\n\t"
    "v_mfma_f32_16x16x32_bf16 v[200:203], v[88:91], v[112:115], 0\n\t"
    "s_mov_b32 s27, 0x20000\n\t"
    "v_lshl_add_u32 v33, v34, 24, v33\n\t"
    "v_mfma_f32_16x16x32_bf16 v[204:207], v[88:91], v[116:119], 0\n\t"
    "s_lshl2_add_u32 s87, s37, s30\n\t"
    "v_lshrrev_b32 v28, 3, v1\n\t"
    "v_mfma_f32_16x16x32_bf16 v[208:211], v[92:95], v[104:107], 0\n\t"
    "s_waitcnt lgkmcnt(0) vmcnt(14)\n\t"
    "s_barrier\n\t"
    "ds_read_b128 v[120:123], v10 offset:0\n\t"
    "v_mfma_f32_16x16x32_bf16 v[212:215], v[92:95], v[108:111], 0\n\t"
    "s_lshl_b32 s84, s87, 14\n\t"
    "buffer_load_dwordx4 v5, s[4:7], s44 offen offset:3072 lds\n\t"
    "ds_read_b64_tr_b16 v[80:81], v11 offset:12288\n\t"
    "v_mfma_f32_16x16x32_bf16 v[216:219], v[92:95], v[112:115], 0\n\t"
    "s_mul_i32 s87, s87, 44032\n\t"
    "ds_read_b64_tr_b16 v[82:83], v11 offset:12544\n\t"
    "v_mfma_f32_16x16x32_bf16 v[220:223], v[92:95], v[116:119], 0\n\t"
    "s_add_u32 m0, s43, 49152\n\t"
    "ds_read_b128 v[124:127], v10 offset:2048\n\t"
    "v_mfma_f32_16x16x32_bf16 v[224:227], v[96:99], v[104:107], 0\n\t"
    "buffer_load_dwordx4 v6, s[8:11], s45 offen offset:0 lds\n\t"
    "ds_read_b128 v[128:131], v10 offset:4096\n\t"
    "v_mfma_f32_16x16x32_bf16 v[228:231], v[96:99], v[108:111], 0\n\t"
    "s_add_u32 s24, s16, s87\n\t"
    "s_addc_u32 s25, s17, 0\n\t"
    "ds_read_b128 v[132:135], v10 offset:6144\n\t"
    "v_mfma_f32_16x16x32_bf16 v[232:235], v[96:99], v[112:115], 0\n\t"
    "s_mov_b32 s28, 0xbfb8aa3b\n\t"
    "ds_read_b64_tr_b16 v[84:85], v11 offset:15360\n\t"
    "v_mfma_f32_16x16x32_bf16 v[236:239], v[96:99], v[116:119], 0\n\t"
    "s_mov_b32 s29, 0xbfb8aa3b\n\t"
    "buffer_load_dwordx4 v7, s[8:11], s45 offen offset:1024 lds\n\t"
    "ds_read_b64_tr_b16 v[86:87], v11 offset:15616\n\t"
    "v_mfma_f32_16x16x32_bf16 v[240:243], v[100:103], v[104:107], 0\n\t"
    "s_mov_b32 s92, 100000\n\t"
    "ds_read_b64_tr_b16 v[88:89], v11 offset:13312\n\t"
    "v_mfma_f32_16x16x32_bf16 v[244:247], v[100:103], v[108:111], 0\n\t"
    "s_lshl_b32 s38, s38, 7\n\t"
    "ds_read_b64_tr_b16 v[90:91], v11 offset:13568\n\t"
    "v_mfma_f32_16x16x32_bf16 v[248:251], v[100:103], v[112:115], 0\n\t"
    "s_add_u32 s84, s84, s38\n\t"
    "buffer_load_dwordx4 v8, s[8:11], s45 offen offset:2048 lds\n\t"
    "ds_read_b64_tr_b16 v[92:93], v11 offset:16384\n\t"
    "v_mfma_f32_16x16x32_bf16 v[252:255], v[100:103], v[116:119], 0\n\t"
    "s_waitcnt lgkmcnt(5)\n\t"
    "ds_read_b64_tr_b16 v[94:95], v11 offset:16640\n\t"
    "v_mfma_f32_16x16x32_bf16 v[160:163], v[80:83], v[120:123], v[160:163]\n\t"
    "s_add_u32 s85, s84, 8192\n\t"
    "ds_read_b64_tr_b16 v[96:97], v11 offset:14336\n\t"
    "v_mfma_f32_16x16x32_bf16 v[164:167], v[80:83], v[124:127], v[164:167]\n\t"
    "s_add_u32 m0, s43, 61440\n\t"
    "ds_read_b64_tr_b16 v[98:99], v11 offset:14592\n\t"
    "v_mfma_f32_16x16x32_bf16 v[168:171], v[80:83], v[128:131], v[168:171]\n\t"
    "s_lshl3_add_u32 s87, s30, 524288\n\t"
    "buffer_load_dwordx4 v6, s[8:11], s46 offen offset:0 lds\n\t"
    "ds_read_b64_tr_b16 v[100:101], v11 offset:17408\n\t"
    "v_mfma_f32_16x16x32_bf16 v[172:175], v[80:83], v[132:135], v[172:175]\n\t"
    "s_waitcnt lgkmcnt(7)\n\t"
    "ds_read_b64_tr_b16 v[102:103], v11 offset:17664\n\t"
    "v_mfma_f32_16x16x32_bf16 v[176:179], v[84:87], v[120:123], v[176:179]\n\t"
    "s_bfe_u32 s86, s41, 196620\n\t"
    "v_and_b32 v30, 7, v1\n\t"
    "v_mfma_f32_16x16x32_bf16 v[180:183], v[84:87], v[124:127], v[180:183]\n\t"
    "s_lshl_b32 s44, s86, 7\n\t"
    "v_and_b32 v31, 6, v28\n\t"
    "v_mfma_f32_16x16x32_bf16 v[184:187], v[84:87], v[128:131], v[184:187]\n\t"
    "s_add_u32 s44, s44, s39\n\t"
    "buffer_load_dwordx4 v7, s[8:11], s46 offen offset:1024 lds\n\t"
    "v_mfma_f32_16x16x32_bf16 v[188:191], v[84:87], v[132:135], v[188:191]\n\t"
    "s_waitcnt lgkmcnt(4)\n\t"
    "v_xor_b32 v31, v30, v31\n\t"
    "v_mfma_f32_16x16x32_bf16 v[192:195], v[88:91], v[120:123], v[192:195]\n\t"
    "s_bfe_u32 s53, 0x120c0600, s87\n\t"
    "v_lshlrev_b32 v31, 4, v31\n\t"
    "v_mfma_f32_16x16x32_bf16 v[196:199], v[88:91], v[124:127], v[196:199]\n\t"
    "s_lshl_b32 s53, s53, 14\n\t"
    "v_lshl_add_u32 v15, v28, 10, v31\n\t"
    "v_mfma_f32_16x16x32_bf16 v[200:203], v[88:91], v[128:131], v[200:203]\n\t"
    "s_bfe_u32 s54, 0x28221c16, s87\n\t"
    "buffer_load_dwordx4 v8, s[8:11], s46 offen offset:2048 lds\n\t"
    "v_mfma_f32_16x16x32_bf16 v[204:207], v[88:91], v[132:135], v[204:207]\n\t"
    "s_lshl_b32 s54, s54, 14\n\t"
    "v_readfirstlane_b32 s80, v24\n\t"
    "v_mfma_f32_16x16x32_bf16 v[208:211], v[92:95], v[120:123], v[208:211]\n\t"
    "s_waitcnt lgkmcnt(0) vmcnt(10)\n\t"
    "s_barrier\n\t"
    "ds_read_b128 v[104:107], v9 offset:16384\n\t"
    "v_mfma_f32_16x16x32_bf16 v[212:215], v[92:95], v[124:127], v[212:215]\n\t"
    "s_add_u32 m0, s42, 0\n\t"
    "ds_read_b64_tr_b16 v[80:81], v11 offset:24576\n\t"
    "v_mfma_f32_16x16x32_bf16 v[216:219], v[92:95], v[128:131], v[216:219]\n\t"
    "s_bfe_u32 s55, 0x33322d2c, s87\n\t"
    "buffer_load_dwordx4 v2, s[4:7], s44 offen offset:0 lds\n\t"
    "ds_read_b64_tr_b16 v[82:83], v11 offset:24832\n\t"
    "v_mfma_f32_16x16x32_bf16 v[220:223], v[92:95], v[132:135], v[220:223]\n\t"
    "s_lshl_b32 s55, s55, 14\n\t"
    "ds_read_b128 v[108:111], v9 offset:18432\n\t"
    "v_mfma_f32_16x16x32_bf16 v[224:227], v[96:99], v[120:123], v[224:227]\n\t"
    "s_mul_i32 s45, s86, 176128\n\t"
    "ds_read_b128 v[112:115], v9 offset:20480\n\t"
    "v_mfma_f32_16x16x32_bf16 v[228:231], v[96:99], v[124:127], v[228:231]\n\t"
    "s_add_u32 s45, s45, s40\n\t"
    "ds_read_b128 v[116:119], v9 offset:22528\n\t"
    "v_mfma_f32_16x16x32_bf16 v[232:235], v[96:99], v[128:131], v[232:235]\n\t"
    "s_add_u32 s46, s45, 88064\n\t"
    "ds_read_b64_tr_b16 v[84:85], v11 offset:27648\n\t"
    "v_mfma_f32_16x16x32_bf16 v[236:239], v[96:99], v[132:135], v[236:239]\n\t"
    "s_bfe_u32 s56, 0x48423e38, s87\n\t"
    "buffer_load_dwordx4 v3, s[4:7], s44 offen offset:1024 lds\n\t"
    "ds_read_b64_tr_b16 v[86:87], v11 offset:27904\n\t"
    "v_mfma_f32_16x16x32_bf16 v[240:243], v[100:103], v[120:123], v[240:243]\n\t"
    "s_lshl_b32 s56, s56, 14\n\t"
    "ds_read_b64_tr_b16 v[88:89], v11 offset:25600\n\t"
    "v_mfma_f32_16x16x32_bf16 v[244:247], v[100:103], v[124:127], v[244:247]\n\t"
    "s_bfe_u32 s57, 0x402544e, s87\n\t"
    "ds_read_b64_tr_b16 v[90:91], v11 offset:25856\n\t"
    "v_mfma_f32_16x16x32_bf16 v[248:251], v[100:103], v[128:131], v[248:251]\n\t"
    "s_lshl_b32 s57, s57, 14\n\t"
    "ds_read_b64_tr_b16 v[92:93], v11 offset:28672\n\t"
    "v_mfma_f32_16x16x32_bf16 v[252:255], v[100:103], v[132:135], v[252:255]\n\t"
    "s_waitcnt lgkmcnt(5)\n\t"
    "ds_read_b64_tr_b16 v[94:95], v11 offset:28928\n\t"
    "v_mfma_f32_16x16x32_bf16 v[160:163], v[80:83], v[104:107], v[160:163]\n\t"
    "s_bfe_u32 s58, 0xb0a0908, s87\n\t"
    "buffer_load_dwordx4 v4, s[4:7], s44 offen offset:2048 lds\n\t"
    "ds_read_b64_tr_b16 v[96:97], v11 offset:26624\n\t"
    "v_mfma_f32_16x16x32_bf16 v[164:167], v[80:83], v[108:111], v[164:167]\n\t"
    "s_lshl_b32 s58, s58, 14\n\t"
    "ds_read_b64_tr_b16 v[98:99], v11 offset:26880\n\t"
    "v_mfma_f32_16x16x32_bf16 v[168:171], v[80:83], v[112:115], v[168:171]\n\t"
    "s_bfe_u32 s59, 0x1814100e, s87\n\t"
    "ds_read_b64_tr_b16 v[100:101], v11 offset:29696\n\t"
    "v_mfma_f32_16x16x32_bf16 v[172:175], v[80:83], v[116:119], v[172:175]\n\t"
    "s_waitcnt lgkmcnt(7)\n\t"
    "ds_read_b64_tr_b16 v[102:103], v11 offset:29952\n\t"
    "v_mfma_f32_16x16x32_bf16 v[176:179], v[84:87], v[104:107], v[176:179]\n\t"
    "s_lshl_b32 s59, s59, 14\n\t"
    "v_readfirstlane_b32 s81, v25\n\t"
    "v_mfma_f32_16x16x32_bf16 v[180:183], v[84:87], v[108:111], v[180:183]\n\t"
    "s_bfe_u32 s60, 0x24201e1a, s87\n\t"
    "buffer_load_dwordx4 v5, s[4:7], s44 offen offset:3072 lds\n\t"
    "v_mfma_f32_16x16x32_bf16 v[184:187], v[84:87], v[112:115], v[184:187]\n\t"
    "s_lshl_b32 s60, s60, 14\n\t"
    "v_lshlrev_b32 v31, 4, v30\n\t"
    "v_mfma_f32_16x16x32_bf16 v[188:191], v[84:87], v[116:119], v[188:191]\n\t"
    "s_waitcnt lgkmcnt(4)\n\t"
    "v_lshl_add_u32 v53, v28, 10, v31\n\t"
    "v_mfma_f32_16x16x32_bf16 v[192:195], v[88:91], v[104:107], v[192:195]\n\t"
    "s_add_u32 m0, s43, 0\n\t"
    "v_xor_b32 v31, v28, v30\n\t"
    "v_mfma_f32_16x16x32_bf16 v[196:199], v[88:91], v[108:111], v[196:199]\n\t"
    "s_bfe_u32 s61, 0x2b2a2726, s87\n\t"
    "buffer_load_dwordx4 v6, s[8:11], s45 offen offset:0 lds\n\t"
    "v_mfma_f32_16x16x32_bf16 v[200:203], v[88:91], v[112:115], v[200:203]\n\t"
    "s_lshl_b32 s61, s61, 14\n\t"
    "v_lshlrev_b32 v31, 4, v31\n\t"
    "v_mfma_f32_16x16x32_bf16 v[204:207], v[88:91], v[116:119], v[204:207]\n\t"
    "s_bfe_u32 s62, 0x3634302e, s87\n\t"
    "v_lshl_add_u32 v52, v28, 7, v31\n\t"
    "v_mfma_f32_16x16x32_bf16 v[208:211], v[92:95], v[104:107], v[208:211]\n\t"
    "s_waitcnt lgkmcnt(0)\n\t"
    "ds_read_b128 v[120:123], v10 offset:16384\n\t"
    "v_mfma_f32_16x16x32_bf16 v[212:215], v[92:95], v[108:111], v[212:215]\n\t"
    "s_lshl_b32 s62, s62, 14\n\t"
    "ds_read_b64_tr_b16 v[80:81], v11 offset:36864\n\t"
    "v_mfma_f32_16x16x32_bf16 v[216:219], v[92:95], v[112:115], v[216:219]\n\t"
    "s_bfe_u32 s63, 0x44403c3a, s87\n\t"
    "buffer_load_dwordx4 v7, s[8:11], s45 offen offset:1024 lds\n\t"
    "ds_read_b64_tr_b16 v[82:83], v11 offset:37120\n\t"
    "v_mfma_f32_16x16x32_bf16 v[220:223], v[92:95], v[116:119], v[220:223]\n\t"
    "s_lshl_b32 s63, s63, 14\n\t"
    "ds_read_b128 v[124:127], v10 offset:18432\n\t"
    "v_mfma_f32_16x16x32_bf16 v[224:227], v[96:99], v[104:107], v[224:227]\n\t"
    "s_bfe_u32 s64, 0x4b4a4746, s87\n\t"
    "ds_read_b128 v[128:131], v10 offset:20480\n\t"
    "v_mfma_f32_16x16x32_bf16 v[228:231], v[96:99], v[108:111], v[228:231]\n\t"
    "s_lshl_b32 s64, s64, 14\n\t"
    "ds_read_b128 v[132:135], v10 offset:22528\n\t"
    "v_mfma_f32_16x16x32_bf16 v[232:235], v[96:99], v[112:115], v[232:235]\n\t"
    "s_bfe_u32 s65, 0x5652504c, s87\n\t"
    "ds_read_b64_tr_b16 v[84:85], v11 offset:39936\n\t"
    "v_mfma_f32_16x16x32_bf16 v[236:239], v[96:99], v[116:119], v[236:239]\n\t"
    "s_lshl_b32 s65, s65, 14\n\t"
    "buffer_load_dwordx4 v8, s[8:11], s45 offen offset:2048 lds\n\t"
    "ds_read_b64_tr_b16 v[86:87], v11 offset:40192\n\t"
    "v_mfma_f32_16x16x32_bf16 v[240:243], v[100:103], v[104:107], v[240:243]\n\t"
    "s_mov_b32 s67, 4096\n\t"
    "ds_read_b64_tr_b16 v[88:89], v11 offset:37888\n\t"
    "v_mfma_f32_16x16x32_bf16 v[244:247], v[100:103], v[108:111], v[244:247]\n\t"
    "s_mov_b32 s68, 8192\n\t"
    "ds_read_b64_tr_b16 v[90:91], v11 offset:38144\n\t"
    "v_mfma_f32_16x16x32_bf16 v[248:251], v[100:103], v[112:115], v[248:251]\n\t"
    "s_mov_b32 s69, 12288\n\t"
    "ds_read_b64_tr_b16 v[92:93], v11 offset:40960\n\t"
    "v_mfma_f32_16x16x32_bf16 v[252:255], v[100:103], v[116:119], v[252:255]\n\t"
    "s_waitcnt lgkmcnt(5)\n\t"
    "s_add_u32 m0, s43, 12288\n\t"
    "ds_read_b64_tr_b16 v[94:95], v11 offset:41216\n\t"
    "v_mfma_f32_16x16x32_bf16 v[160:163], v[80:83], v[120:123], v[160:163]\n\t"
    "s_mov_b32 s70, 16384\n\t"
    "buffer_load_dwordx4 v6, s[8:11], s46 offen offset:0 lds\n\t"
    "ds_read_b64_tr_b16 v[96:97], v11 offset:38912\n\t"
    "v_mfma_f32_16x16x32_bf16 v[164:167], v[80:83], v[124:127], v[164:167]\n\t"
    "s_mov_b32 s71, 20480\n\t"
    "ds_read_b64_tr_b16 v[98:99], v11 offset:39168\n\t"
    "v_mfma_f32_16x16x32_bf16 v[168:171], v[80:83], v[128:131], v[168:171]\n\t"
    "s_mov_b32 s72, 24576\n\t"
    "ds_read_b64_tr_b16 v[100:101], v11 offset:41984\n\t"
    "v_mfma_f32_16x16x32_bf16 v[172:175], v[80:83], v[132:135], v[172:175]\n\t"
    "s_waitcnt lgkmcnt(7)\n\t"
    "ds_read_b64_tr_b16 v[102:103], v11 offset:42240\n\t"
    "v_mfma_f32_16x16x32_bf16 v[176:179], v[84:87], v[120:123], v[176:179]\n\t"
    "s_bfe_u32 s86, s41, 196624\n\t"
    "v_lshrrev_b32 v29, 5, v1\n\t"
    "v_mfma_f32_16x16x32_bf16 v[180:183], v[84:87], v[124:127], v[180:183]\n\t"
    "s_lshl_b32 s44, s86, 7\n\t"
    "buffer_load_dwordx4 v7, s[8:11], s46 offen offset:1024 lds\n\t"
    "v_mfma_f32_16x16x32_bf16 v[184:187], v[84:87], v[128:131], v[184:187]\n\t"
    "s_add_u32 s44, s44, s39\n\t"
    "v_and_b32 v32, 31, v1\n\t"
    "v_mfma_f32_16x16x32_bf16 v[188:191], v[84:87], v[132:135], v[188:191]\n\t"
    "s_waitcnt lgkmcnt(4)\n\t"
    "v_mul_u32_u24 v31, 44032, v29\n\t"
    "v_mfma_f32_16x16x32_bf16 v[192:195], v[88:91], v[120:123], v[192:195]\n\t"
    "s_mov_b32 s73, 28672\n\t"
    "v_lshl_add_u32 v19, v32, 4, v31\n\t"
    "v_mfma_f32_16x16x32_bf16 v[196:199], v[88:91], v[124:127], v[196:199]\n\t"
    "s_mov_b32 s74, 32768\n\t"
    "buffer_load_dwordx4 v8, s[8:11], s46 offen offset:2048 lds\n\t"
    "v_mfma_f32_16x16x32_bf16 v[200:203], v[88:91], v[128:131], v[200:203]\n\t"
    "s_mov_b32 s75, 36864\n\t"
    "v_lshlrev_b32 v22, 4, v1\n\t"
    "v_mfma_f32_16x16x32_bf16 v[204:207], v[88:91], v[132:135], v[204:207]\n\t"
    "s_mov_b32 s76, 40960\n\t"
    "v_lshlrev_b32 v31, 11, v29\n\t"
    "v_mfma_f32_16x16x32_bf16 v[208:211], v[92:95], v[120:123], v[208:211]\n\t"
    "s_waitcnt lgkmcnt(0) vmcnt(10)\n\t"
    "s_barrier\n\t"
    "ds_read_b128 v[104:107], v9 offset:32768\n\t"
    "v_mfma_f32_16x16x32_bf16 v[212:215], v[92:95], v[124:127], v[212:215]\n\t"
    "s_add_u32 m0, s42, 16384\n\t"
    "ds_read_b64_tr_b16 v[80:81], v12 offset:0\n\t"
    "v_mfma_f32_16x16x32_bf16 v[216:219], v[92:95], v[128:131], v[216:219]\n\t"
    "s_lshl1_add_u32 s87, s79, s31\n\t"
    "buffer_load_dwordx4 v2, s[4:7], s44 offen offset:0 lds\n\t"
    "ds_read_b64_tr_b16 v[82:83], v12 offset:256\n\t"
    "v_mfma_f32_16x16x32_bf16 v[220:223], v[92:95], v[132:135], v[220:223]\n\t"
    "s_mul_i32 s88, s87, 344\n\t"
    "ds_read_b128 v[108:111], v9 offset:34816\n\t"
    "v_mfma_f32_16x16x32_bf16 v[224:227], v[96:99], v[120:123], v[224:227]\n\t"
    "s_mul_i32 s45, s86, 176128\n\t"
    "ds_read_b128 v[112:115], v9 offset:36864\n\t"
    "v_mfma_f32_16x16x32_bf16 v[228:231], v[96:99], v[124:127], v[228:231]\n\t"
    "s_add_u32 s45, s45, s40\n\t"
    "ds_read_b128 v[116:119], v9 offset:38912\n\t"
    "v_mfma_f32_16x16x32_bf16 v[232:235], v[96:99], v[128:131], v[232:235]\n\t"
    "s_add_u32 s46, s45, 88064\n\t"
    "ds_read_b64_tr_b16 v[84:85], v12 offset:3072\n\t"
    "v_mfma_f32_16x16x32_bf16 v[236:239], v[96:99], v[132:135], v[236:239]\n\t"
    "s_add_u32 s88, s88, s78\n\t"
    "buffer_load_dwordx4 v3, s[4:7], s44 offen offset:1024 lds\n\t"
    "ds_read_b64_tr_b16 v[86:87], v12 offset:3328\n\t"
    "v_mfma_f32_16x16x32_bf16 v[240:243], v[100:103], v[120:123], v[240:243]\n\t"
    "s_lshl1_add_u32 s77, s34, s88\n\t"
    "ds_read_b64_tr_b16 v[88:89], v12 offset:1024\n\t"
    "v_mfma_f32_16x16x32_bf16 v[244:247], v[100:103], v[124:127], v[244:247]\n\t"
    "s_lshl_b32 s77, s77, 9\n\t"
    "ds_read_b64_tr_b16 v[90:91], v12 offset:1280\n\t"
    "v_mfma_f32_16x16x32_bf16 v[248:251], v[100:103], v[128:131], v[248:251]\n\t"
    "s_mul_i32 s89, s48, 7\n\t"
    "ds_read_b64_tr_b16 v[92:93], v12 offset:4096\n\t"
    "v_mfma_f32_16x16x32_bf16 v[252:255], v[100:103], v[132:135], v[252:255]\n\t"
    "s_waitcnt lgkmcnt(5)\n\t"
    "ds_read_b64_tr_b16 v[94:95], v12 offset:4352\n\t"
    "v_mfma_f32_16x16x32_bf16 v[160:163], v[80:83], v[104:107], v[160:163]\n\t"
    "s_lshl1_add_u32 s89, s49, s89\n\t"
    "buffer_load_dwordx4 v4, s[4:7], s44 offen offset:2048 lds\n\t"
    "ds_read_b64_tr_b16 v[96:97], v12 offset:2048\n\t"
    "v_mfma_f32_16x16x32_bf16 v[164:167], v[80:83], v[108:111], v[164:167]\n\t"
    "s_lshl1_add_u32 s52, s89, s88\n\t"
    "ds_read_b64_tr_b16 v[98:99], v12 offset:2304\n\t"
    "v_mfma_f32_16x16x32_bf16 v[168:171], v[80:83], v[112:115], v[168:171]\n\t"
    "s_lshl_b32 s52, s52, 9\n\t"
    "ds_read_b64_tr_b16 v[100:101], v12 offset:5120\n\t"
    "v_mfma_f32_16x16x32_bf16 v[172:175], v[80:83], v[116:119], v[172:175]\n\t"
    "s_waitcnt lgkmcnt(7)\n\t"
    "ds_read_b64_tr_b16 v[102:103], v12 offset:5376\n\t"
    "v_mfma_f32_16x16x32_bf16 v[176:179], v[84:87], v[104:107], v[176:179]\n\t"
    "s_lshl_b32 s87, s87, 9\n\t"
    "v_bfe_u32 v28, v1, 2, 3\n\t"
    "v_mfma_f32_16x16x32_bf16 v[180:183], v[84:87], v[108:111], v[180:183]\n\t"
    "s_lshl1_add_u32 s88, s34, s78\n\t"
    "buffer_load_dwordx4 v5, s[4:7], s44 offen offset:3072 lds\n\t"
    "v_mfma_f32_16x16x32_bf16 v[184:187], v[84:87], v[112:115], v[184:187]\n\t"
    "s_add_u32 s87, s87, s88\n\t"
    "v_lshl_add_u32 v31, v28, 7, v31\n\t"
    "v_mfma_f32_16x16x32_bf16 v[188:191], v[84:87], v[116:119], v[188:191]\n\t"
    "s_waitcnt lgkmcnt(4)\n\t"
    "v_bfe_u32 v28, v1, 3, 2\n\t"
    "v_mfma_f32_16x16x32_bf16 v[192:195], v[88:91], v[104:107], v[192:195]\n\t"
    "s_add_u32 m0, s43, 24576\n\t"
    "v_lshl_add_u32 v31, v28, 5, v31\n\t"
    "v_mfma_f32_16x16x32_bf16 v[196:199], v[88:91], v[108:111], v[196:199]\n\t"
    "s_lshl3_add_u32 s87, s87, 6291456\n\t"
    "buffer_load_dwordx4 v6, s[8:11], s45 offen offset:0 lds\n\t"
    "v_mfma_f32_16x16x32_bf16 v[200:203], v[88:91], v[112:115], v[200:203]\n\t"
    "s_lshl_b32 s88, s93, 9\n\t"
    "v_and_b32 v28, 3, v1\n\t"
    "v_mfma_f32_16x16x32_bf16 v[204:207], v[88:91], v[116:119], v[204:207]\n\t"
    "s_add_u32 s88, s88, 6291456\n\t"
    "v_lshl_add_u32 v36, v28, 3, v31\n\t"
    "v_mfma_f32_16x16x32_bf16 v[208:211], v[92:95], v[104:107], v[208:211]\n\t"
    "s_waitcnt lgkmcnt(0)\n\t"
    "ds_read_b128 v[120:123], v10 offset:32768\n\t"
    "v_mfma_f32_16x16x32_bf16 v[212:215], v[92:95], v[108:111], v[212:215]\n\t"
    "s_lshl_b32 s89, s30, 11\n\t"
    "ds_read_b64_tr_b16 v[80:81], v12 offset:12288\n\t"
    "v_mfma_f32_16x16x32_bf16 v[216:219], v[92:95], v[112:115], v[216:219]\n\t"
    "s_add_u32 s89, s89, 139264\n\t"
    "buffer_load_dwordx4 v7, s[8:11], s45 offen offset:1024 lds\n\t"
    "ds_read_b64_tr_b16 v[82:83], v12 offset:12544\n\t"
    "v_mfma_f32_16x16x32_bf16 v[220:223], v[92:95], v[116:119], v[220:223]\n\t"
    "ds_read_b128 v[124:127], v10 offset:34816\n\t"
    "v_mfma_f32_16x16x32_bf16 v[224:227], v[96:99], v[104:107], v[224:227]\n\t"
    "ds_read_b128 v[128:131], v10 offset:36864\n\t"
    "v_mfma_f32_16x16x32_bf16 v[228:231], v[96:99], v[108:111], v[228:231]\n\t"
    "ds_read_b128 v[132:135], v10 offset:38912\n\t"
    "v_mfma_f32_16x16x32_bf16 v[232:235], v[96:99], v[112:115], v[232:235]\n\t"
    "ds_read_b64_tr_b16 v[84:85], v12 offset:15360\n\t"
    "v_mfma_f32_16x16x32_bf16 v[236:239], v[96:99], v[116:119], v[236:239]\n\t"
    "buffer_load_dwordx4 v8, s[8:11], s45 offen offset:2048 lds\n\t"
    "ds_read_b64_tr_b16 v[86:87], v12 offset:15616\n\t"
    "v_mfma_f32_16x16x32_bf16 v[240:243], v[100:103], v[104:107], v[240:243]\n\t"
    "ds_read_b64_tr_b16 v[88:89], v12 offset:13312\n\t"
    "v_mfma_f32_16x16x32_bf16 v[244:247], v[100:103], v[108:111], v[244:247]\n\t"
    "ds_read_b64_tr_b16 v[90:91], v12 offset:13568\n\t"
    "v_mfma_f32_16x16x32_bf16 v[248:251], v[100:103], v[112:115], v[248:251]\n\t"
    "ds_read_b64_tr_b16 v[92:93], v12 offset:16384\n\t"
    "v_mfma_f32_16x16x32_bf16 v[252:255], v[100:103], v[116:119], v[252:255]\n\t"
    "s_waitcnt lgkmcnt(5)\n\t"
    "s_add_u32 m0, s43, 36864\n\t"
    "ds_read_b64_tr_b16 v[94:95], v12 offset:16640\n\t"
    "v_mfma_f32_16x16x32_bf16 v[160:163], v[80:83], v[120:123], v[160:163]\n\t"
    "buffer_load_dwordx4 v6, s[8:11], s46 offen offset:0 lds\n\t"
    "ds_read_b64_tr_b16 v[96:97], v12 offset:14336\n\t"
    "v_mfma_f32_16x16x32_bf16 v[164:167], v[80:83], v[124:127], v[164:167]\n\t"
    "ds_read_b64_tr_b16 v[98:99], v12 offset:14592\n\t"
    "v_mfma_f32_16x16x32_bf16 v[168:171], v[80:83], v[128:131], v[168:171]\n\t"
    "ds_read_b64_tr_b16 v[100:101], v12 offset:17408\n\t"
    "v_mfma_f32_16x16x32_bf16 v[172:175], v[80:83], v[132:135], v[172:175]\n\t"
    "s_waitcnt lgkmcnt(7)\n\t"
    "ds_read_b64_tr_b16 v[102:103], v12 offset:17664\n\t"
    "v_mfma_f32_16x16x32_bf16 v[176:179], v[84:87], v[120:123], v[176:179]\n\t"
    "s_bfe_u32 s86, s41, 196628\n\t"
    "v_xor_b32 v37, 32, v36\n\t"
    "v_mfma_f32_16x16x32_bf16 v[180:183], v[84:87], v[124:127], v[180:183]\n\t"
    "s_lshl_b32 s44, s86, 7\n\t"
    "buffer_load_dwordx4 v7, s[8:11], s46 offen offset:1024 lds\n\t"
    "v_mfma_f32_16x16x32_bf16 v[184:187], v[84:87], v[128:131], v[184:187]\n\t"
    "s_add_u32 s44, s44, s39\n\t"
    "v_xor_b32 v38, 64, v36\n\t"
    "v_mfma_f32_16x16x32_bf16 v[188:191], v[84:87], v[132:135], v[188:191]\n\t"
    "s_waitcnt lgkmcnt(4)\n\t"
    "v_xor_b32 v39, 96, v36\n\t"
    "v_mfma_f32_16x16x32_bf16 v[192:195], v[88:91], v[120:123], v[192:195]\n\t"
    "v_add_u32 v40, 65536, v36\n\t"
    "v_mfma_f32_16x16x32_bf16 v[196:199], v[88:91], v[124:127], v[196:199]\n\t"
    "L_hop1_%=:\n\t"
    "s_cbranch_vccnz L_hopback%=\n\t"
    "buffer_load_dwordx4 v8, s[8:11], s46 offen offset:2048 lds\n\t"
    "v_mfma_f32_16x16x32_bf16 v[200:203], v[88:91], v[128:131], v[200:203]\n\t"
    "v_add_u32 v41, 65536, v37\n\t"
    "v_mfma_f32_16x16x32_bf16 v[204:207], v[88:91], v[132:135], v[204:207]\n\t"
    "v_add_u32 v42, 65536, v38\n\t"
    "v_mfma_f32_16x16x32_bf16 v[208:211], v[92:95], v[120:123], v[208:211]\n\t"
    "s_waitcnt lgkmcnt(0) vmcnt(10)\n\t"
    "s_barrier\n\t"
    "ds_read_b128 v[104:107], v9 offset:0\n\t"
    "v_mfma_f32_16x16x32_bf16 v[212:215], v[92:95], v[124:127], v[212:215]\n\t"
    "s_add_u32 m0, s42, 32768\n\t"
    "buffer_load_dword v14, v33, s[12:15], s51 offen\n\t"
    "ds_read_b64_tr_b16 v[80:81], v11 offset:0\n\t"
    "v_mfma_f32_16x16x32_bf16 v[216:219], v[92:95], v[128:131], v[216:219]\n\t"
    "buffer_load_dwordx4 v2, s[4:7], s44 offen offset:0 lds\n\t"
    "ds_read_b64_tr_b16 v[82:83], v11 offset:256\n\t"
    "v_mfma_f32_16x16x32_bf16 v[220:223], v[92:95], v[132:135], v[220:223]\n\t"
    "ds_read_b128 v[108:111], v9 offset:2048\n\t"
    "v_mfma_f32_16x16x32_bf16 v[224:227], v[96:99], v[120:123], v[224:227]\n\t"
    "s_mul_i32 s45, s86, 176128\n\t"
    "ds_read_b128 v[112:115], v9 offset:4096\n\t"
    "v_mfma_f32_16x16x32_bf16 v[228:231], v[96:99], v[124:127], v[228:231]\n\t"
    "s_add_u32 s45, s45, s40\n\t"
    "ds_read_b128 v[116:119], v9 offset:6144\n\t"
    "v_mfma_f32_16x16x32_bf16 v[232:235], v[96:99], v[128:131], v[232:235]\n\t"
    "s_add_u32 s46, s45, 88064\n\t"
    "ds_read_b64_tr_b16 v[84:85], v11 offset:3072\n\t"
    "v_mfma_f32_16x16x32_bf16 v[236:239], v[96:99], v[132:135], v[236:239]\n\t"
    "buffer_load_dwordx4 v3, s[4:7], s44 offen offset:1024 lds\n\t"
    "ds_read_b64_tr_b16 v[86:87], v11 offset:3328\n\t"
    "v_mfma_f32_16x16x32_bf16 v[240:243], v[100:103], v[120:123], v[240:243]\n\t"
    "ds_read_b64_tr_b16 v[88:89], v11 offset:1024\n\t"
    "v_mfma_f32_16x16x32_bf16 v[244:247], v[100:103], v[124:127], v[244:247]\n\t"
    "ds_read_b64_tr_b16 v[90:91], v11 offset:1280\n\t"
    "v_mfma_f32_16x16x32_bf16 v[248:251], v[100:103], v[128:131], v[248:251]\n\t"
    "ds_read_b64_tr_b16 v[92:93], v11 offset:4096\n\t"
    "v_mfma_f32_16x16x32_bf16 v[252:255], v[100:103], v[132:135], v[252:255]\n\t"
    "s_waitcnt lgkmcnt(5)\n\t"
    "ds_read_b64_tr_b16 v[94:95], v11 offset:4352\n\t"
    "v_mfma_f32_16x16x32_bf16 v[160:163], v[80:83], v[104:107], v[160:163]\n\t"
    "buffer_load_dwordx4 v4, s[4:7], s44 offen offset:2048 lds\n\t"
    "ds_read_b64_tr_b16 v[96:97], v11 offset:2048\n\t"
    "v_mfma_f32_16x16x32_bf16 v[164:167], v[80:83], v[108:111], v[164:167]\n\t"
    "ds_read_b64_tr_b16 v[98:99], v11 offset:2304\n\t"
    "v_mfma_f32_16x16x32_bf16 v[168:171], v[80:83], v[112:115], v[168:171]\n\t"
    "ds_read_b64_tr_b16 v[100:101], v11 offset:5120\n\t"
    "v_mfma_f32_16x16x32_bf16 v[172:175], v[80:83], v[116:119], v[172:175]\n\t"
    "s_waitcnt lgkmcnt(7)\n\t"
    "ds_read_b64_tr_b16 v[102:103], v11 offset:5376\n\t"
    "v_mfma_f32_16x16x32_bf16 v[176:179], v[84:87], v[104:107], v[176:179]\n\t"
    "v_add_u32 v43, 65536, v39\n\t"
    "v_mfma_f32_16x16x32_bf16 v[180:183], v[84:87], v[108:111], v[180:183]\n\t"
    "buffer_load_dwordx4 v5, s[4:7], s44 offen offset:3072 lds\n\t"
    "v_mfma_f32_16x16x32_bf16 v[184:187], v[84:87], v[112:115], v[184:187]\n\t"
    "v_add_u32 v44, 131072, v36\n\t"
    "v_mfma_f32_16x16x32_bf16 v[188:191], v[84:87], v[116:119], v[188:191]\n\t"
    "s_waitcnt lgkmcnt(4)\n\t"
    "v_add_u32 v45, 131072, v37\n\t"
    "v_mfma_f32_16x16x32_bf16 v[192:195], v[88:91], v[104:107], v[192:195]\n\t"
    "s_add_u32 m0, s43, 49152\n\t"
    "v_add_u32 v46, 131072, v38\n\t"
    "v_mfma_f32_16x16x32_bf16 v[196:199], v[88:91], v[108:111], v[196:199]\n\t"
    "buffer_load_dwordx4 v6, s[8:11], s45 offen offset:0 lds\n\t"
    "v_mfma_f32_16x16x32_bf16 v[200:203], v[88:91], v[112:115], v[200:203]\n\t"
    "v_add_u32 v47, 131072, v39\n\t"
    "v_mfma_f32_16x16x32_bf16 v[204:207], v[88:91], v[116:119], v[204:207]\n\t"
    "v_xor_b32 v28, v29, v30\n\t"
    "v_mfma_f32_16x16x32_bf16 v[208:211], v[92:95], v[104:107], v[208:211]\n\t"
    "s_waitcnt lgkmcnt(0)\n\t"
    "ds_read_b128 v[120:123], v10 offset:0\n\t"
    "v_mfma_f32_16x16x32_bf16 v[212:215], v[92:95], v[108:111], v[212:215]\n\t"
    "ds_read_b64_tr_b16 v[80:81], v11 offset:12288\n\t"
    "v_mfma_f32_16x16x32_bf16 v[216:219], v[92:95], v[112:115], v[216:219]\n\t"
    "buffer_load_dwordx4 v7, s[8:11], s45 offen offset:1024 lds\n\t"
    "ds_read_b64_tr_b16 v[82:83], v11 offset:12544\n\t"
    "v_mfma_f32_16x16x32_bf16 v[220:223], v[92:95], v[116:119], v[220:223]\n\t"
    "ds_read_b128 v[124:127], v10 offset:2048\n\t"
    "v_mfma_f32_16x16x32_bf16 v[224:227], v[96:99], v[104:107], v[224:227]\n\t"
    "ds_read_b128 v[128:131], v10 offset:4096\n\t"
    "v_mfma_f32_16x16x32_bf16 v[228:231], v[96:99], v[108:111], v[228:231]\n\t"
    "ds_read_b128 v[132:135], v10 offset:6144\n\t"
    "v_mfma_f32_16x16x32_bf16 v[232:235], v[96:99], v[112:115], v[232:235]\n\t"
    "ds_read_b64_tr_b16 v[84:85], v11 offset:15360\n\t"
    "v_mfma_f32_16x16x32_bf16 v[236:239], v[96:99], v[116:119], v[236:239]\n\t"
    "buffer_load_dwordx4 v8, s[8:11], s45 offen offset:2048 lds\n\t"
    "ds_read_b64_tr_b16 v[86:87], v11 offset:15616\n\t"
    "v_mfma_f32_16x16x32_bf16 v[240:243], v[100:103], v[104:107], v[240:243]\n\t"
    "ds_read_b64_tr_b16 v[88:89], v11 offset:13312\n\t"
    "v_mfma_f32_16x16x32_bf16 v[244:247], v[100:103], v[108:111], v[244:247]\n\t"
    "ds_read_b64_tr_b16 v[90:91], v11 offset:13568\n\t"
    "v_mfma_f32_16x16x32_bf16 v[248:251], v[100:103], v[112:115], v[248:251]\n\t"
    "ds_read_b64_tr_b16 v[92:93], v11 offset:16384\n\t"
    "v_mfma_f32_16x16x32_bf16 v[252:255], v[100:103], v[116:119], v[252:255]\n\t"
    "s_waitcnt lgkmcnt(5)\n\t"
    "s_add_u32 m0, s43, 61440\n\t"
    "ds_read_b64_tr_b16 v[94:95], v11 offset:16640\n\t"
    "v_mfma_f32_16x16x32_bf16 v[160:163], v[80:83], v[120:123], v[160:163]\n\t"
    "buffer_load_dwordx4 v6, s[8:11], s46 offen offset:0 lds\n\t"
    "ds_read_b64_tr_b16 v[96:97], v11 offset:14336\n\t"
    "v_mfma_f32_16x16x32_bf16 v[164:167], v[80:83], v[124:127], v[164:167]\n\t"
    "ds_read_b64_tr_b16 v[98:99], v11 offset:14592\n\t"
    "v_mfma_f32_16x16x32_bf16 v[168:171], v[80:83], v[128:131], v[168:171]\n\t"
    "ds_read_b64_tr_b16 v[100:101], v11 offset:17408\n\t"
    "v_mfma_f32_16x16x32_bf16 v[172:175], v[80:83], v[132:135], v[172:175]\n\t"
    "s_waitcnt lgkmcnt(7)\n\t"
    "ds_read_b64_tr_b16 v[102:103], v11 offset:17664\n\t"
    "v_mfma_f32_16x16x32_bf16 v[176:179], v[84:87], v[120:123], v[176:179]\n\t"
    "s_bfe_u32 s86, s41, 196632\n\t"
    "v_and_b32 v31, 15, v1\n\t"
    "v_mfma_f32_16x16x32_bf16 v[180:183], v[84:87], v[124:127], v[180:183]\n\t"
    "s_lshl_b32 s44, s86, 7\n\t"
    "buffer_load_dwordx4 v7, s[8:11], s46 offen offset:1024 lds\n\t"
    "v_mfma_f32_16x16x32_bf16 v[184:187], v[84:87], v[128:131], v[184:187]\n\t"
    "s_add_u32 s44, s44, s39\n\t"
    "v_lshlrev_b32 v31, 7, v31\n\t"
    "v_mfma_f32_16x16x32_bf16 v[188:191], v[84:87], v[132:135], v[188:191]\n\t"
    "s_waitcnt lgkmcnt(4)\n\t"
    "v_lshl_add_u32 v31, v28, 4, v31\n\t"
    "v_mfma_f32_16x16x32_bf16 v[192:195], v[88:91], v[120:123], v[192:195]\n\t"
    "v_bfe_u32 v28, v1, 4, 1\n\t"
    "v_mfma_f32_16x16x32_bf16 v[196:199], v[88:91], v[124:127], v[196:199]\n\t"
    "buffer_load_dwordx4 v8, s[8:11], s46 offen offset:2048 lds\n\t"
    "v_mfma_f32_16x16x32_bf16 v[200:203], v[88:91], v[128:131], v[200:203]\n\t"
    "v_lshl_add_u32 v48, v28, 3, v31\n\t"
    "v_mfma_f32_16x16x32_bf16 v[204:207], v[88:91], v[132:135], v[204:207]\n\t"
    "v_add_u32 v15, s38, v15\n\t"
    "v_mfma_f32_16x16x32_bf16 v[208:211], v[92:95], v[120:123], v[208:211]\n\t"
    "s_waitcnt lgkmcnt(0) vmcnt(10)\n\t"
    "s_barrier\n\t"
    "ds_read_b128 v[104:107], v9 offset:16384\n\t"
    "v_mfma_f32_16x16x32_bf16 v[212:215], v[92:95], v[124:127], v[212:215]\n\t"
    "s_add_u32 m0, s42, 0\n\t"
    "ds_read_b64_tr_b16 v[80:81], v11 offset:24576\n\t"
    "v_mfma_f32_16x16x32_bf16 v[216:219], v[92:95], v[128:131], v[216:219]\n\t"
    "buffer_load_dwordx4 v2, s[4:7], s44 offen offset:0 lds\n\t"
    "ds_read_b64_tr_b16 v[82:83], v11 offset:24832\n\t"
    "v_mfma_f32_16x16x32_bf16 v[220:223], v[92:95], v[132:135], v[220:223]\n\t"
    "ds_read_b128 v[108:111], v9 offset:18432\n\t"
    "v_mfma_f32_16x16x32_bf16 v[224:227], v[96:99], v[120:123], v[224:227]\n\t"
    "s_mul_i32 s45, s86, 176128\n\t"
    "ds_read_b128 v[112:115], v9 offset:20480\n\t"
    "v_mfma_f32_16x16x32_bf16 v[228:231], v[96:99], v[124:127], v[228:231]\n\t"
    "s_add_u32 s45, s45, s40\n\t"
    "ds_read_b128 v[116:119], v9 offset:22528\n\t"
    "v_mfma_f32_16x16x32_bf16 v[232:235], v[96:99], v[128:131], v[232:235]\n\t"
    "s_add_u32 s46, s45, 88064\n\t"
    "ds_read_b64_tr_b16 v[84:85], v11 offset:27648\n\t"
    "v_mfma_f32_16x16x32_bf16 v[236:239], v[96:99], v[132:135], v[236:239]\n\t"
    "buffer_load_dwordx4 v3, s[4:7], s44 offen offset:1024 lds\n\t"
    "ds_read_b64_tr_b16 v[86:87], v11 offset:27904\n\t"
    "v_mfma_f32_16x16x32_bf16 v[240:243], v[100:103], v[120:123], v[240:243]\n\t"
    "ds_read_b64_tr_b16 v[88:89], v11 offset:25600\n\t"
    "v_mfma_f32_16x16x32_bf16 v[244:247], v[100:103], v[124:127], v[244:247]\n\t"
    "ds_read_b64_tr_b16 v[90:91], v11 offset:25856\n\t"
    "v_mfma_f32_16x16x32_bf16 v[248:251], v[100:103], v[128:131], v[248:251]\n\t"
    "ds_read_b64_tr_b16 v[92:93], v11 offset:28672\n\t"
    "v_mfma_f32_16x16x32_bf16 v[252:255], v[100:103], v[132:135], v[252:255]\n\t"
    "s_waitcnt lgkmcnt(5)\n\t"
    "ds_read_b64_tr_b16 v[94:95], v11 offset:28928\n\t"
    "v_mfma_f32_16x16x32_bf16 v[160:163], v[80:83], v[104:107], v[160:163]\n\t"
    "buffer_load_dwordx4 v4, s[4:7], s44 offen offset:2048 lds\n\t"
    "ds_read_b64_tr_b16 v[96:97], v11 offset:26624\n\t"
    "v_mfma_f32_16x16x32_bf16 v[164:167], v[80:83], v[108:111], v[164:167]\n\t"
    "ds_read_b64_tr_b16 v[98:99], v11 offset:26880\n\t"
    "v_mfma_f32_16x16x32_bf16 v[168:171], v[80:83], v[112:115], v[168:171]\n\t"
    "ds_read_b64_tr_b16 v[100:101], v11 offset:29696\n\t"
    "v_mfma_f32_16x16x32_bf16 v[172:175], v[80:83], v[116:119], v[172:175]\n\t"
    "s_waitcnt lgkmcnt(7)\n\t"
    "ds_read_b64_tr_b16 v[102:103], v11 offset:29952\n\t"
    "v_mfma_f32_16x16x32_bf16 v[176:179], v[84:87], v[104:107], v[176:179]\n\t"
    "v_add_u32 v16, 7168, v15\n\t"
    "v_mfma_f32_16x16x32_bf16 v[180:183], v[84:87], v[108:111], v[180:183]\n\t"
    "buffer_load_dwordx4 v5, s[4:7], s44 offen offset:3072 lds\n\t"
    "v_mfma_f32_16x16x32_bf16 v[184:187], v[84:87], v[112:115], v[184:187]\n\t"
    "v_add_u32 v17, 14336, v15\n\t"
    "v_mfma_f32_16x16x32_bf16 v[188:191], v[84:87], v[116:119], v[188:191]\n\t"
    "s_waitcnt lgkmcnt(4)\n\t"
    "v_add_u32 v18, 21504, v15\n\t"
    "v_mfma_f32_16x16x32_bf16 v[192:195], v[88:91], v[104:107], v[192:195]\n\t"
    "s_add_u32 m0, s43, 0\n\t"
    "v_lshl_add_u32 v20, v30, 9, s87\n\t"
    "v_mfma_f32_16x16x32_bf16 v[196:199], v[88:91], v[108:111], v[196:199]\n\t"
    "buffer_load_dwordx4 v6, s[8:11], s45 offen offset:0 lds\n\t"
    "v_mfma_f32_16x16x32_bf16 v[200:203], v[88:91], v[112:115], v[200:203]\n\t"
    "v_lshl_add_u32 v21, v32, 3, s88\n\t"
    "v_mfma_f32_16x16x32_bf16 v[204:207], v[88:91], v[116:119], v[204:207]\n\t"
    "v_add_u32 v52, s89, v52\n\t"
    "v_mfma_f32_16x16x32_bf16 v[208:211], v[92:95], v[104:107], v[208:211]\n\t"
    "s_waitcnt lgkmcnt(0)\n\t"
    "ds_read_b128 v[120:123], v10 offset:16384\n\t"
    "v_mfma_f32_16x16x32_bf16 v[212:215], v[92:95], v[108:111], v[212:215]\n\t"
    "ds_read_b64_tr_b16 v[80:81], v11 offset:36864\n\t"
    "v_mfma_f32_16x16x32_bf16 v[216:219], v[92:95], v[112:115], v[216:219]\n\t"
    "buffer_load_dwordx4 v7, s[8:11], s45 offen offset:1024 lds\n\t"
    "ds_read_b64_tr_b16 v[82:83], v11 offset:37120\n\t"
    "v_mfma_f32_16x16x32_bf16 v[220:223], v[92:95], v[116:119], v[220:223]\n\t"
    "ds_read_b128 v[124:127], v10 offset:18432\n\t"
    "v_mfma_f32_16x16x32_bf16 v[224:227], v[96:99], v[104:107], v[224:227]\n\t"
    "ds_read_b128 v[128:131], v10 offset:20480\n\t"
    "v_mfma_f32_16x16x32_bf16 v[228:231], v[96:99], v[108:111], v[228:231]\n\t"
    "ds_read_b128 v[132:135], v10 offset:22528\n\t"
    "v_mfma_f32_16x16x32_bf16 v[232:235], v[96:99], v[112:115], v[232:235]\n\t"
    "ds_read_b64_tr_b16 v[84:85], v11 offset:39936\n\t"
    "v_mfma_f32_16x16x32_bf16 v[236:239], v[96:99], v[116:119], v[236:239]\n\t"
    "buffer_load_dwordx4 v8, s[8:11], s45 offen offset:2048 lds\n\t"
    "ds_read_b64_tr_b16 v[86:87], v11 offset:40192\n\t"
    "v_mfma_f32_16x16x32_bf16 v[240:243], v[100:103], v[104:107], v[240:243]\n\t"
    "ds_read_b64_tr_b16 v[88:89], v11 offset:37888\n\t"
    "v_mfma_f32_16x16x32_bf16 v[244:247], v[100:103], v[108:111], v[244:247]\n\t"
    "ds_read_b64_tr_b16 v[90:91], v11 offset:38144\n\t"
    "v_mfma_f32_16x16x32_bf16 v[248:251], v[100:103], v[112:115], v[248:251]\n\t"
    "ds_read_b64_tr_b16 v[92:93], v11 offset:40960\n\t"
    "v_mfma_f32_16x16x32_bf16 v[252:255], v[100:103], v[116:119], v[252:255]\n\t"
    "s_waitcnt lgkmcnt(5)\n\t"
    "s_add_u32 m0, s43, 12288\n\t"
    "ds_read_b64_tr_b16 v[94:95], v11 offset:41216\n\t"
    "v_mfma_f32_16x16x32_bf16 v[160:163], v[80:83], v[120:123], v[160:163]\n\t"
    "buffer_load_dwordx4 v6, s[8:11], s46 offen offset:0 lds\n\t"
    "ds_read_b64_tr_b16 v[96:97], v11 offset:38912\n\t"
    "v_mfma_f32_16x16x32_bf16 v[164:167], v[80:83], v[124:127], v[164:167]\n\t"
    "ds_read_b64_tr_b16 v[98:99], v11 offset:39168\n\t"
    "v_mfma_f32_16x16x32_bf16 v[168:171], v[80:83], v[128:131], v[168:171]\n\t"
    "ds_read_b64_tr_b16 v[100:101], v11 offset:41984\n\t"
    "v_mfma_f32_16x16x32_bf16 v[172:175], v[80:83], v[132:135], v[172:175]\n\t"
    "s_waitcnt lgkmcnt(7)\n\t"
    "ds_read_b64_tr_b16 v[102:103], v11 offset:42240\n\t"
    "v_mfma_f32_16x16x32_bf16 v[176:179], v[84:87], v[120:123], v[176:179]\n\t"
    "s_bfe_u32 s86, s41, 196636\n\t"
    "v_add_u32 v48, s89, v48\n\t"
    "v_mfma_f32_16x16x32_bf16 v[180:183], v[84:87], v[124:127], v[180:183]\n\t"
    "s_lshl_b32 s44, s86, 7\n\t"
    "buffer_load_dwordx4 v7, s[8:11], s46 offen offset:1024 lds\n\t"
    "v_mfma_f32_16x16x32_bf16 v[184:187], v[84:87], v[128:131], v[184:187]\n\t"
    "s_add_u32 s44, s44, s39\n\t"
    "v_xor_b32 v49, 32, v48\n\t"
    "v_mfma_f32_16x16x32_bf16 v[188:191], v[84:87], v[132:135], v[188:191]\n\t"
    "s_waitcnt lgkmcnt(4)\n\t"
    "v_xor_b32 v50, 64, v48\n\t"
    "v_mfma_f32_16x16x32_bf16 v[192:195], v[88:91], v[120:123], v[192:195]\n\t"
    "v_xor_b32 v51, 96, v48\n\t"
    "v_mfma_f32_16x16x32_bf16 v[196:199], v[88:91], v[124:127], v[196:199]\n\t"
    "buffer_load_dwordx4 v8, s[8:11], s46 offen offset:2048 lds\n\t"
    "v_mfma_f32_16x16x32_bf16 v[200:203], v[88:91], v[128:131], v[200:203]\n\t"
    "v_mov_b32 v54, 0\n\t"
    "v_mfma_f32_16x16x32_bf16 v[204:207], v[88:91], v[132:135], v[204:207]\n\t"
    "v_mov_b32 v55, 0\n\t"
    "v_mfma_f32_16x16x32_bf16 v[208:211], v[92:95], v[120:123], v[208:211]\n\t"
    "s_waitcnt lgkmcnt(0) vmcnt(10)\n\t"
    "s_barrier\n\t"
    "ds_read_b128 v[104:107], v9 offset:32768\n\t"
    "v_mfma_f32_16x16x32_bf16 v[212:215], v[92:95], v[124:127], v[212:215]\n\t"
    "s_add_u32 m0, s42, 16384\n\t"
    "ds_read_b64_tr_b16 v[80:81], v12 offset:0\n\t"
    "v_mfma_f32_16x16x32_bf16 v[216:219], v[92:95], v[128:131], v[216:219]\n\t"
    "buffer_load_dwordx4 v2, s[4:7], s44 offen offset:0 lds\n\t"
    "ds_read_b64_tr_b16 v[82:83], v12 offset:256\n\t"
    "v_mfma_f32_16x16x32_bf16 v[220:223], v[92:95], v[132:135], v[220:223]\n\t"
    "ds_read_b128 v[108:111], v9 offset:34816\n\t"
    "v_mfma_f32_16x16x32_bf16 v[224:227], v[96:99], v[120:123], v[224:227]\n\t"
    "s_mul_i32 s45, s86, 176128\n\t"
    "ds_read_b128 v[112:115], v9 offset:36864\n\t"
    "v_mfma_f32_16x16x32_bf16 v[228:231], v[96:99], v[124:127], v[228:231]\n\t"
    "s_add_u32 s45, s45, s40\n\t"
    "ds_read_b128 v[116:119], v9 offset:38912\n\t"
    "v_mfma_f32_16x16x32_bf16 v[232:235], v[96:99], v[128:131], v[232:235]\n\t"
    "s_add_u32 s46, s45, 88064\n\t"
    "ds_read_b64_tr_b16 v[84:85], v12 offset:3072\n\t"
    "v_mfma_f32_16x16x32_bf16 v[236:239], v[96:99], v[132:135], v[236:239]\n\t"
    "buffer_load_dwordx4 v3, s[4:7], s44 offen offset:1024 lds\n\t"
    "ds_read_b64_tr_b16 v[86:87], v12 offset:3328\n\t"
    "v_mfma_f32_16x16x32_bf16 v[240:243], v[100:103], v[120:123], v[240:243]\n\t"
    "ds_read_b64_tr_b16 v[88:89], v12 offset:1024\n\t"
    "v_mfma_f32_16x16x32_bf16 v[244:247], v[100:103], v[124:127], v[244:247]\n\t"
    "ds_read_b64_tr_b16 v[90:91], v12 offset:1280\n\t"
    "v_mfma_f32_16x16x32_bf16 v[248:251], v[100:103], v[128:131], v[248:251]\n\t"
    "ds_read_b64_tr_b16 v[92:93], v12 offset:4096\n\t"
    "v_mfma_f32_16x16x32_bf16 v[252:255], v[100:103], v[132:135], v[252:255]\n\t"
    "s_waitcnt lgkmcnt(5)\n\t"
    "ds_read_b64_tr_b16 v[94:95], v12 offset:4352\n\t"
    "v_mfma_f32_16x16x32_bf16 v[160:163], v[80:83], v[104:107], v[160:163]\n\t"
    "buffer_load_dwordx4 v4, s[4:7], s44 offen offset:2048 lds\n\t"
    "ds_read_b64_tr_b16 v[96:97], v12 offset:2048\n\t"
    "v_mfma_f32_16x16x32_bf16 v[164:167], v[80:83], v[108:111], v[164:167]\n\t"
    "ds_read_b64_tr_b16 v[98:99], v12 offset:2304\n\t"
    "v_mfma_f32_16x16x32_bf16 v[168:171], v[80:83], v[112:115], v[168:171]\n\t"
    "ds_read_b64_tr_b16 v[100:101], v12 offset:5120\n\t"
    "v_mfma_f32_16x16x32_bf16 v[172:175], v[80:83], v[116:119], v[172:175]\n\t"
    "s_waitcnt lgkmcnt(7)\n\t"
    "ds_read_b64_tr_b16 v[102:103], v12 offset:5376\n\t"
    "v_mfma_f32_16x16x32_bf16 v[176:179], v[84:87], v[104:107], v[176:179]\n\t"
    "v_mov_b32 v56, 0\n\t"
    "v_mfma_f32_16x16x32_bf16 v[180:183], v[84:87], v[108:111], v[180:183]\n\t"
    "buffer_load_dwordx4 v5, s[4:7], s44 offen offset:3072 lds\n\t"
    "v_mfma_f32_16x16x32_bf16 v[184:187], v[84:87], v[112:115], v[184:187]\n\t"
    "v_mov_b32 v57, 0\n\t"
    "v_mfma_f32_16x16x32_bf16 v[188:191], v[84:87], v[116:119], v[188:191]\n\t"
    "s_waitcnt lgkmcnt(4)\n\t"
    "v_mov_b32 v58, 0\n\t"
    "v_mfma_f32_16x16x32_bf16 v[192:195], v[88:91], v[104:107], v[192:195]\n\t"
    "s_add_u32 m0, s43, 24576\n\t"
    "v_mov_b32 v59, 0\n\t"
    "v_mfma_f32_16x16x32_bf16 v[196:199], v[88:91], v[108:111], v[196:199]\n\t"
    "buffer_load_dwordx4 v6, s[8:11], s45 offen offset:0 lds\n\t"
    "v_mfma_f32_16x16x32_bf16 v[200:203], v[88:91], v[112:115], v[200:203]\n\t"
    "v_mfma_f32_16x16x32_bf16 v[204:207], v[88:91], v[116:119], v[204:207]\n\t"
    "v_mfma_f32_16x16x32_bf16 v[208:211], v[92:95], v[104:107], v[208:211]\n\t"
    "s_waitcnt lgkmcnt(0)\n\t"
    "ds_read_b128 v[120:123], v10 offset:32768\n\t"
    "v_mfma_f32_16x16x32_bf16 v[212:215], v[92:95], v[108:111], v[212:215]\n\t"
    "ds_read_b64_tr_b16 v[80:81], v12 offset:12288\n\t"
    "v_mfma_f32_16x16x32_bf16 v[216:219], v[92:95], v[112:115], v[216:219]\n\t"
    "buffer_load_dwordx4 v7, s[8:11], s45 offen offset:1024 lds\n\t"
    "ds_read_b64_tr_b16 v[82:83], v12 offset:12544\n\t"
    "v_mfma_f32_16x16x32_bf16 v[220:223], v[92:95], v[116:119], v[220:223]\n\t"
    "ds_read_b128 v[124:127], v10 offset:34816\n\t"
    "v_mfma_f32_16x16x32_bf16 v[224:227], v[96:99], v[104:107], v[224:227]\n\t"
    "ds_read_b128 v[128:131], v10 offset:36864\n\t"
    "v_mfma_f32_16x16x32_bf16 v[228:231], v[96:99], v[108:111], v[228:231]\n\t"
    "ds_read_b128 v[132:135], v10 offset:38912\n\t"
    "v_mfma_f32_16x16x32_bf16 v[232:235], v[96:99], v[112:115], v[232:235]\n\t"
    "ds_read_b64_tr_b16 v[84:85], v12 offset:15360\n\t"
    "v_mfma_f32_16x16x32_bf16 v[236:239], v[96:99], v[116:119], v[236:239]\n\t"
    "buffer_load_dwordx4 v8, s[8:11], s45 offen offset:2048 lds\n\t"
    "ds_read_b64_tr_b16 v[86:87], v12 offset:15616\n\t"
    "v_mfma_f32_16x16x32_bf16 v[240:243], v[100:103], v[104:107], v[240:243]\n\t"
    "ds_read_b64_tr_b16 v[88:89], v12 offset:13312\n\t"
    "v_mfma_f32_16x16x32_bf16 v[244:247], v[100:103], v[108:111], v[244:247]\n\t"
    "ds_read_b64_tr_b16 v[90:91], v12 offset:13568\n\t"
    "v_mfma_f32_16x16x32_bf16 v[248:251], v[100:103], v[112:115], v[248:251]\n\t"
    "ds_read_b64_tr_b16 v[92:93], v12 offset:16384\n\t"
    "v_mfma_f32_16x16x32_bf16 v[252:255], v[100:103], v[116:119], v[252:255]\n\t"
    "s_waitcnt lgkmcnt(5)\n\t"
    "s_add_u32 m0, s43, 36864\n\t"
    "ds_read_b64_tr_b16 v[94:95], v12 offset:16640\n\t"
    "v_mfma_f32_16x16x32_bf16 v[160:163], v[80:83], v[120:123], v[160:163]\n\t"
    "buffer_load_dwordx4 v6, s[8:11], s46 offen offset:0 lds\n\t"
    "ds_read_b64_tr_b16 v[96:97], v12 offset:14336\n\t"
    "v_mfma_f32_16x16x32_bf16 v[164:167], v[80:83], v[124:127], v[164:167]\n\t"
    "ds_read_b64_tr_b16 v[98:99], v12 offset:14592\n\t"
    "v_mfma_f32_16x16x32_bf16 v[168:171], v[80:83], v[128:131], v[168:171]\n\t"
    "ds_read_b64_tr_b16 v[100:101], v12 offset:17408\n\t"
    "v_mfma_f32_16x16x32_bf16 v[172:175], v[80:83], v[132:135], v[172:175]\n\t"
    "s_waitcnt lgkmcnt(7)\n\t"
    "ds_read_b64_tr_b16 v[102:103], v12 offset:17664\n\t"
    "v_mfma_f32_16x16x32_bf16 v[176:179], v[84:87], v[120:123], v[176:179]\n\t"
    "v_mfma_f32_16x16x32_bf16 v[180:183], v[84:87], v[124:127], v[180:183]\n\t"
    "buffer_load_dwordx4 v7, s[8:11], s46 offen offset:1024 lds\n\t"
    "v_mfma_f32_16x16x32_bf16 v[184:187], v[84:87], v[128:131], v[184:187]\n\t"
    "v_mfma_f32_16x16x32_bf16 v[188:191], v[84:87], v[132:135], v[188:191]\n\t"
    "s_waitcnt lgkmcnt(4)\n\t"
    "v_mfma_f32_16x16x32_bf16 v[192:195], v[88:91], v[120:123], v[192:195]\n\t"
    "v_mfma_f32_16x16x32_bf16 v[196:199], v[88:91], v[124:127], v[196:199]\n\t"
    "buffer_load_dwordx4 v8, s[8:11], s46 offen offset:2048 lds\n\t"
    "v_mfma_f32_16x16x32_bf16 v[200:203], v[88:91], v[128:131], v[200:203]\n\t"
    "v_mfma_f32_16x16x32_bf16 v[204:207], v[88:91], v[132:135], v[204:207]\n\t"
    "v_mfma_f32_16x16x32_bf16 v[208:211], v[92:95], v[120:123], v[208:211]\n\t"
    "s_waitcnt lgkmcnt(0) vmcnt(10)\n\t"
    "s_barrier\n\t"
    "ds_read_b128 v[104:107], v9 offset:0\n\t"
    "v_mfma_f32_16x16x32_bf16 v[212:215], v[92:95], v[124:127], v[212:215]\n\t"
    "s_add_u32 m0, s42, 49152\n\t"
    "ds_read_b64_tr_b16 v[80:81], v11 offset:0\n\t"
    "v_mfma_f32_16x16x32_bf16 v[216:219], v[92:95], v[128:131], v[216:219]\n\t"
    "buffer_load_dwordx4 v15, s[12:15], s53 offen offset:0 lds\n\t"
    "ds_read_b64_tr_b16 v[82:83], v11 offset:256\n\t"
    "v_mfma_f32_16x16x32_bf16 v[220:223], v[92:95], v[132:135], v[220:223]\n\t"
    "ds_read_b128 v[108:111], v9 offset:2048\n\t"
    "v_mfma_f32_16x16x32_bf16 v[224:227], v[96:99], v[120:123], v[224:227]\n\t"
    "ds_read_b128 v[112:115], v9 offset:4096\n\t"
    "v_mfma_f32_16x16x32_bf16 v[228:231], v[96:99], v[124:127], v[228:231]\n\t"
    "ds_read_b128 v[116:119], v9 offset:6144\n\t"
    "v_mfma_f32_16x16x32_bf16 v[232:235], v[96:99], v[128:131], v[232:235]\n\t"
    "ds_read_b64_tr_b16 v[84:85], v11 offset:3072\n\t"
    "v_mfma_f32_16x16x32_bf16 v[236:239], v[96:99], v[132:135], v[236:239]\n\t"
    "buffer_load_dwordx4 v16, s[12:15], s53 offen offset:1024 lds\n\t"
    "ds_read_b64_tr_b16 v[86:87], v11 offset:3328\n\t"
    "v_mfma_f32_16x16x32_bf16 v[240:243], v[100:103], v[120:123], v[240:243]\n\t"
    "ds_read_b64_tr_b16 v[88:89], v11 offset:1024\n\t"
    "v_mfma_f32_16x16x32_bf16 v[244:247], v[100:103], v[124:127], v[244:247]\n\t"
    "ds_read_b64_tr_b16 v[90:91], v11 offset:1280\n\t"
    "v_mfma_f32_16x16x32_bf16 v[248:251], v[100:103], v[128:131], v[248:251]\n\t"
    "ds_read_b64_tr_b16 v[92:93], v11 offset:4096\n\t"
    "v_mfma_f32_16x16x32_bf16 v[252:255], v[100:103], v[132:135], v[252:255]\n\t"
    "s_waitcnt lgkmcnt(5)\n\t"
    "ds_read_b64_tr_b16 v[94:95], v11 offset:4352\n\t"
    "v_mfma_f32_16x16x32_bf16 v[160:163], v[80:83], v[104:107], v[160:163]\n\t"
    "buffer_load_dwordx4 v17, s[12:15], s53 offen offset:2048 lds\n\t"
    "ds_read_b64_tr_b16 v[96:97], v11 offset:2048\n\t"
    "v_mfma_f32_16x16x32_bf16 v[164:167], v[80:83], v[108:111], v[164:167]\n\t"
    "ds_read_b64_tr_b16 v[98:99], v11 offset:2304\n\t"
    "v_mfma_f32_16x16x32_bf16 v[168:171], v[80:83], v[112:115], v[168:171]\n\t"
    "ds_read_b64_tr_b16 v[100:101], v11 offset:5120\n\t"
    "v_mfma_f32_16x16x32_bf16 v[172:175], v[80:83], v[116:119], v[172:175]\n\t"
    "s_waitcnt lgkmcnt(7)\n\t"
    "ds_read_b64_tr_b16 v[102:103], v11 offset:5376\n\t"
    "v_mfma_f32_16x16x32_bf16 v[176:179], v[84:87], v[104:107], v[176:179]\n\t"
    "v_mfma_f32_16x16x32_bf16 v[180:183], v[84:87], v[108:111], v[180:183]\n\t"
    "s_lshr_b32 s43, s42, 1\n\t"
    "buffer_load_dwordx4 v18, s[12:15], s53 offen offset:3072 lds\n\t"
    "v_mfma_f32_16x16x32_bf16 v[184:187], v[84:87], v[112:115], v[184:187]\n\t"
    "v_mfma_f32_16x16x32_bf16 v[188:191], v[84:87], v[116:119], v[188:191]\n\t"
    "s_waitcnt lgkmcnt(4)\n\t"
    "v_mfma_f32_16x16x32_bf16 v[192:195], v[88:91], v[104:107], v[192:195]\n\t"
    "s_add_u32 m0, s42, 139264\n\t"
    "v_mfma_f32_16x16x32_bf16 v[196:199], v[88:91], v[108:111], v[196:199]\n\t"
    "buffer_load_dwordx4 v15, s[12:15], s54 offen offset:0 lds\n\t"
    "v_mfma_f32_16x16x32_bf16 v[200:203], v[88:91], v[112:115], v[200:203]\n\t"
    "v_mfma_f32_16x16x32_bf16 v[204:207], v[88:91], v[116:119], v[204:207]\n\t"
    "v_mfma_f32_16x16x32_bf16 v[208:211], v[92:95], v[104:107], v[208:211]\n\t"
    "s_waitcnt lgkmcnt(0)\n\t"
    "ds_read_b128 v[120:123], v10 offset:0\n\t"
    "v_mfma_f32_16x16x32_bf16 v[212:215], v[92:95], v[108:111], v[212:215]\n\t"
    "ds_read_b64_tr_b16 v[80:81], v11 offset:12288\n\t"
    "v_mfma_f32_16x16x32_bf16 v[216:219], v[92:95], v[112:115], v[216:219]\n\t"
    "buffer_load_dwordx4 v16, s[12:15], s54 offen offset:1024 lds\n\t"
    "ds_read_b64_tr_b16 v[82:83], v11 offset:12544\n\t"
    "v_mfma_f32_16x16x32_bf16 v[220:223], v[92:95], v[116:119], v[220:223]\n\t"
    "ds_read_b128 v[124:127], v10 offset:2048\n\t"
    "v_mfma_f32_16x16x32_bf16 v[224:227], v[96:99], v[104:107], v[224:227]\n\t"
    "ds_read_b128 v[128:131], v10 offset:4096\n\t"
    "v_mfma_f32_16x16x32_bf16 v[228:231], v[96:99], v[108:111], v[228:231]\n\t"
    "ds_read_b128 v[132:135], v10 offset:6144\n\t"
    "v_mfma_f32_16x16x32_bf16 v[232:235], v[96:99], v[112:115], v[232:235]\n\t"
    "ds_read_b64_tr_b16 v[84:85], v11 offset:15360\n\t"
    "v_mfma_f32_16x16x32_bf16 v[236:239], v[96:99], v[116:119], v[236:239]\n\t"
    "buffer_load_dwordx4 v17, s[12:15], s54 offen offset:2048 lds\n\t"
    "ds_read_b64_tr_b16 v[86:87], v11 offset:15616\n\t"
    "v_mfma_f32_16x16x32_bf16 v[240:243], v[100:103], v[104:107], v[240:243]\n\t"
    "ds_read_b64_tr_b16 v[88:89], v11 offset:13312\n\t"
    "v_mfma_f32_16x16x32_bf16 v[244:247], v[100:103], v[108:111], v[244:247]\n\t"
    "ds_read_b64_tr_b16 v[90:91], v11 offset:13568\n\t"
    "v_mfma_f32_16x16x32_bf16 v[248:251], v[100:103], v[112:115], v[248:251]\n\t"
    "ds_read_b64_tr_b16 v[92:93], v11 offset:16384\n\t"
    "v_mfma_f32_16x16x32_bf16 v[252:255], v[100:103], v[116:119], v[252:255]\n\t"
    "s_waitcnt lgkmcnt(5)\n\t"
    "ds_read_b64_tr_b16 v[94:95], v11 offset:16640\n\t"
    "v_mfma_f32_16x16x32_bf16 v[160:163], v[80:83], v[120:123], v[160:163]\n\t"
    "buffer_load_dwordx4 v18, s[12:15], s54 offen offset:3072 lds\n\t"
    "ds_read_b64_tr_b16 v[96:97], v11 offset:14336\n\t"
    "v_mfma_f32_16x16x32_bf16 v[164:167], v[80:83], v[124:127], v[164:167]\n\t"
    "ds_read_b64_tr_b16 v[98:99], v11 offset:14592\n\t"
    "v_mfma_f32_16x16x32_bf16 v[168:171], v[80:83], v[128:131], v[168:171]\n\t"
    "ds_read_b64_tr_b16 v[100:101], v11 offset:17408\n\t"
    "v_mfma_f32_16x16x32_bf16 v[172:175], v[80:83], v[132:135], v[172:175]\n\t"
    "s_waitcnt lgkmcnt(7)\n\t"
    "ds_read_b64_tr_b16 v[102:103], v11 offset:17664\n\t"
    "v_mfma_f32_16x16x32_bf16 v[176:179], v[84:87], v[120:123], v[176:179]\n\t"
    "s_add_u32 m0, s43, 155648\n\t"
    "v_mfma_f32_16x16x32_bf16 v[180:183], v[84:87], v[124:127], v[180:183]\n\t"
    "buffer_load_dwordx4 v15, s[12:15], s55 offen offset:0 lds\n\t"
    "v_mfma_f32_16x16x32_bf16 v[184:187], v[84:87], v[128:131], v[184:187]\n\t"
    "v_mfma_f32_16x16x32_bf16 v[188:191], v[84:87], v[132:135], v[188:191]\n\t"
    "s_waitcnt lgkmcnt(4)\n\t"
    "v_mfma_f32_16x16x32_bf16 v[192:195], v[88:91], v[120:123], v[192:195]\n\t"
    "v_mfma_f32_16x16x32_bf16 v[196:199], v[88:91], v[124:127], v[196:199]\n\t"
    "buffer_load_dwordx4 v16, s[12:15], s55 offen offset:1024 lds\n\t"
    "v_mfma_f32_16x16x32_bf16 v[200:203], v[88:91], v[128:131], v[200:203]\n\t"
    "v_mfma_f32_16x16x32_bf16 v[204:207], v[88:91], v[132:135], v[204:207]\n\t"
    "v_mfma_f32_16x16x32_bf16 v[208:211], v[92:95], v[120:123], v[208:211]\n\t"
    "s_waitcnt lgkmcnt(0) vmcnt(13)\n\t"
    "s_barrier\n\t"
    "ds_read_b128 v[104:107], v9 offset:16384\n\t"
    "v_mfma_f32_16x16x32_bf16 v[212:215], v[92:95], v[124:127], v[212:215]\n\t"
    "ds_read_b64_tr_b16 v[80:81], v11 offset:24576\n\t"
    "v_mfma_f32_16x16x32_bf16 v[216:219], v[92:95], v[128:131], v[216:219]\n\t"
    "ds_read_b64_tr_b16 v[82:83], v11 offset:24832\n\t"
    "v_mfma_f32_16x16x32_bf16 v[220:223], v[92:95], v[132:135], v[220:223]\n\t"
    "ds_read_b128 v[108:111], v9 offset:18432\n\t"
    "v_mfma_f32_16x16x32_bf16 v[224:227], v[96:99], v[120:123], v[224:227]\n\t"
    "s_add_u32 m0, s42, 32768\n\t"
    "ds_read_b128 v[112:115], v9 offset:20480\n\t"
    "v_mfma_f32_16x16x32_bf16 v[228:231], v[96:99], v[124:127], v[228:231]\n\t"
    "buffer_load_dwordx4 v15, s[12:15], s56 offen offset:0 lds\n\t"
    "ds_read_b128 v[116:119], v9 offset:22528\n\t"
    "v_mfma_f32_16x16x32_bf16 v[232:235], v[96:99], v[128:131], v[232:235]\n\t"
    "ds_read_b64_tr_b16 v[84:85], v11 offset:27648\n\t"
    "v_mfma_f32_16x16x32_bf16 v[236:239], v[96:99], v[132:135], v[236:239]\n\t"
    "ds_read_b64_tr_b16 v[86:87], v11 offset:27904\n\t"
    "v_mfma_f32_16x16x32_bf16 v[240:243], v[100:103], v[120:123], v[240:243]\n\t"
    "ds_read_b64_tr_b16 v[88:89], v11 offset:25600\n\t"
    "v_mfma_f32_16x16x32_bf16 v[244:247], v[100:103], v[124:127], v[244:247]\n\t"
    "ds_read_b64_tr_b16 v[90:91], v11 offset:25856\n\t"
    "v_mfma_f32_16x16x32_bf16 v[248:251], v[100:103], v[128:131], v[248:251]\n\t"
    "buffer_load_dwordx4 v16, s[12:15], s56 offen offset:1024 lds\n\t"
    "ds_read_b64_tr_b16 v[92:93], v11 offset:28672\n\t"
    "v_mfma_f32_16x16x32_bf16 v[252:255], v[100:103], v[132:135], v[252:255]\n\t"
    "s_waitcnt lgkmcnt(5)\n\t"
    "ds_read_b64_tr_b16 v[94:95], v11 offset:28928\n\t"
    "v_mfma_f32_16x16x32_bf16 v[160:163], v[80:83], v[104:107], v[160:163]\n\t"
    "ds_read_b64_tr_b16 v[96:97], v11 offset:26624\n\t"
    "v_mfma_f32_16x16x32_bf16 v[164:167], v[80:83], v[108:111], v[164:167]\n\t"
    "ds_read_b64_tr_b16 v[98:99], v11 offset:26880\n\t"
    "v_mfma_f32_16x16x32_bf16 v[168:171], v[80:83], v[112:115], v[168:171]\n\t"
    "ds_read_b64_tr_b16 v[100:101], v11 offset:29696\n\t"
    "v_mfma_f32_16x16x32_bf16 v[172:175], v[80:83], v[116:119], v[172:175]\n\t"
    "s_waitcnt lgkmcnt(7)\n\t"
    "buffer_load_dwordx4 v17, s[12:15], s56 offen offset:2048 lds\n\t"
    "ds_read_b64_tr_b16 v[102:103], v11 offset:29952\n\t"
    "v_mfma_f32_16x16x32_bf16 v[176:179], v[84:87], v[104:107], v[176:179]\n\t"
    "v_mfma_f32_16x16x32_bf16 v[180:183], v[84:87], v[108:111], v[180:183]\n\t"
    "v_mfma_f32_16x16x32_bf16 v[184:187], v[84:87], v[112:115], v[184:187]\n\t"
    "v_mfma_f32_16x16x32_bf16 v[188:191], v[84:87], v[116:119], v[188:191]\n\t"
    "s_waitcnt lgkmcnt(4)\n\t"
    "v_mfma_f32_16x16x32_bf16 v[192:195], v[88:91], v[104:107], v[192:195]\n\t"
    "buffer_load_dwordx4 v18, s[12:15], s56 offen offset:3072 lds\n\t"
    "v_mfma_f32_16x16x32_bf16 v[196:199], v[88:91], v[108:111], v[196:199]\n\t"
    "v_mfma_f32_16x16x32_bf16 v[200:203], v[88:91], v[112:115], v[200:203]\n\t"
    "v_mfma_f32_16x16x32_bf16 v[204:207], v[88:91], v[116:119], v[204:207]\n\t"
    "v_mfma_f32_16x16x32_bf16 v[208:211], v[92:95], v[104:107], v[208:211]\n\t"
    "s_waitcnt lgkmcnt(0) vmcnt(14)\n\t"
    "s_barrier\n\t"
    "ds_read_b128 v[120:123], v10 offset:16384\n\t"
    "v_mfma_f32_16x16x32_bf16 v[212:215], v[92:95], v[108:111], v[212:215]\n\t"
    "s_add_u32 m0, s42, 114688\n\t"
    "ds_read_b64_tr_b16 v[80:81], v11 offset:36864\n\t"
    "v_mfma_f32_16x16x32_bf16 v[216:219], v[92:95], v[112:115], v[216:219]\n\t"
    "buffer_load_dwordx4 v15, s[12:15], s57 offen offset:0 lds\n\t"
    "ds_read_b64_tr_b16 v[82:83], v11 offset:37120\n\t"
    "v_mfma_f32_16x16x32_bf16 v[220:223], v[92:95], v[116:119], v[220:223]\n\t"
    "ds_read_b128 v[124:127], v10 offset:18432\n\t"
    "v_mfma_f32_16x16x32_bf16 v[224:227], v[96:99], v[104:107], v[224:227]\n\t"
    "ds_read_b128 v[128:131], v10 offset:20480\n\t"
    "v_mfma_f32_16x16x32_bf16 v[228:231], v[96:99], v[108:111], v[228:231]\n\t"
    "ds_read_b128 v[132:135], v10 offset:22528\n\t"
    "v_mfma_f32_16x16x32_bf16 v[232:235], v[96:99], v[112:115], v[232:235]\n\t"
    "buffer_load_dwordx4 v16, s[12:15], s57 offen offset:1024 lds\n\t"
    "ds_read_b64_tr_b16 v[84:85], v11 offset:39936\n\t"
    "v_mfma_f32_16x16x32_bf16 v[236:239], v[96:99], v[116:119], v[236:239]\n\t"
    "ds_read_b64_tr_b16 v[86:87], v11 offset:40192\n\t"
    "v_mfma_f32_16x16x32_bf16 v[240:243], v[100:103], v[104:107], v[240:243]\n\t"
    "ds_read_b64_tr_b16 v[88:89], v11 offset:37888\n\t"
    "v_mfma_f32_16x16x32_bf16 v[244:247], v[100:103], v[108:111], v[244:247]\n\t"
    "ds_read_b64_tr_b16 v[90:91], v11 offset:38144\n\t"
    "v_mfma_f32_16x16x32_bf16 v[248:251], v[100:103], v[112:115], v[248:251]\n\t"
    "ds_read_b64_tr_b16 v[92:93], v11 offset:40960\n\t"
    "v_mfma_f32_16x16x32_bf16 v[252:255], v[100:103], v[116:119], v[252:255]\n\t"
    "s_waitcnt lgkmcnt(5)\n\t"
    "buffer_load_dwordx4 v17, s[12:15], s57 offen offset:2048 lds\n\t"
    "ds_read_b64_tr_b16 v[94:95], v11 offset:41216\n\t"
    "v_mfma_f32_16x16x32_bf16 v[160:163], v[80:83], v[120:123], v[160:163]\n\t"
    "ds_read_b64_tr_b16 v[96:97], v11 offset:38912\n\t"
    "v_mfma_f32_16x16x32_bf16 v[164:167], v[80:83], v[124:127], v[164:167]\n\t"
    "ds_read_b64_tr_b16 v[98:99], v11 offset:39168\n\t"
    "v_mfma_f32_16x16x32_bf16 v[168:171], v[80:83], v[128:131], v[168:171]\n\t"
    "ds_read_b64_tr_b16 v[100:101], v11 offset:41984\n\t"
    "v_mfma_f32_16x16x32_bf16 v[172:175], v[80:83], v[132:135], v[172:175]\n\t"
    "L_hop2_%=:\n\t"
    "s_cbranch_vccnz L_hopback%=\n\t"
    "s_waitcnt lgkmcnt(7)\n\t"
    "ds_read_b64_tr_b16 v[102:103], v11 offset:42240\n\t"
    "v_mfma_f32_16x16x32_bf16 v[176:179], v[84:87], v[120:123], v[176:179]\n\t"
    "buffer_load_dwordx4 v18, s[12:15], s57 offen offset:3072 lds\n\t"
    "v_mfma_f32_16x16x32_bf16 v[180:183], v[84:87], v[124:127], v[180:183]\n\t"
    "v_mfma_f32_16x16x32_bf16 v[184:187], v[84:87], v[128:131], v[184:187]\n\t"
    "v_mfma_f32_16x16x32_bf16 v[188:191], v[84:87], v[132:135], v[188:191]\n\t"
    "s_waitcnt lgkmcnt(4)\n\t"
    "v_mfma_f32_16x16x32_bf16 v[192:195], v[88:91], v[120:123], v[192:195]\n\t"
    "s_add_u32 m0, s43, 131072\n\t"
    "v_mfma_f32_16x16x32_bf16 v[196:199], v[88:91], v[124:127], v[196:199]\n\t"
    "buffer_load_dwordx4 v15, s[12:15], s58 offen offset:0 lds\n\t"
    "v_mfma_f32_16x16x32_bf16 v[200:203], v[88:91], v[128:131], v[200:203]\n\t"
    "v_mfma_f32_16x16x32_bf16 v[204:207], v[88:91], v[132:135], v[204:207]\n\t"
    "v_mfma_f32_16x16x32_bf16 v[208:211], v[92:95], v[120:123], v[208:211]\n\t"
    "s_waitcnt lgkmcnt(0)\n\t"
    "v_mfma_f32_16x16x32_bf16 v[212:215], v[92:95], v[124:127], v[212:215]\n\t"
    "v_mfma_f32_16x16x32_bf16 v[216:219], v[92:95], v[128:131], v[216:219]\n\t"
    "buffer_load_dwordx4 v16, s[12:15], s58 offen offset:1024 lds\n\t"
    "v_mfma_f32_16x16x32_bf16 v[220:223], v[92:95], v[132:135], v[220:223]\n\t"
    "v_mfma_f32_16x16x32_bf16 v[224:227], v[96:99], v[120:123], v[224:227]\n\t"
    "v_mfma_f32_16x16x32_bf16 v[228:231], v[96:99], v[124:127], v[228:231]\n\t"
    "v_mfma_f32_16x16x32_bf16 v[232:235], v[96:99], v[128:131], v[232:235]\n\t"
    "s_add_u32 m0, s42, 0\n\t"
    "v_mfma_f32_16x16x32_bf16 v[236:239], v[96:99], v[132:135], v[236:239]\n\t"
    "buffer_load_dwordx4 v15, s[12:15], s59 offen offset:0 lds\n\t"
    "v_mfma_f32_16x16x32_bf16 v[240:243], v[100:103], v[120:123], v[240:243]\n\t"
    "v_mfma_f32_16x16x32_bf16 v[244:247], v[100:103], v[124:127], v[244:247]\n\t"
    "v_mfma_f32_16x16x32_bf16 v[248:251], v[100:103], v[128:131], v[248:251]\n\t"
    "buffer_load_dwordx4 v16, s[12:15], s59 offen offset:1024 lds\n\t"
    "v_mfma_f32_16x16x32_bf16 v[252:255], v[100:103], v[132:135], v[252:255]\n\t"
    "v_pk_mul_f32 v[136:137], v[160:161], s[28:29]\n\t"
    "v_pk_mul_f32 v[138:139], v[162:163], s[28:29]\n\t"
    "v_pk_mul_f32 v[140:141], v[164:165], s[28:29]\n\t"
    "v_pk_mul_f32 v[142:143], v[166:167], s[28:29]\n\t"
    "v_exp_f32 v136, v136\n\t"
    "v_exp_f32 v137, v137\n\t"
    "v_exp_f32 v138, v138\n\t"
    "v_exp_f32 v139, v139\n\t"
    "v_exp_f32 v140, v140\n\t"
    "v_exp_f32 v141, v141\n\t"
    "v_exp_f32 v142, v142\n\t"
    "v_exp_f32 v143, v143\n\t"
    "v_pk_add_f32 v[136:137], v[136:137], 1.0 op_sel_hi:[1,0]\n\t"
    "v_pk_add_f32 v[138:139], v[138:139], 1.0 op_sel_hi:[1,0]\n\t"
    "v_pk_add_f32 v[140:141], v[140:141], 1.0 op_sel_hi:[1,0]\n\t"
    "v_pk_add_f32 v[142:143], v[142:143], 1.0 op_sel_hi:[1,0]\n\t"
    "v_rcp_f32 v136, v136\n\t"
    "v_rcp_f32 v137, v137\n\t"
    "v_rcp_f32 v138, v138\n\t"
    "v_rcp_f32 v139, v139\n\t"
    "v_rcp_f32 v140, v140\n\t"
    "v_rcp_f32 v141, v141\n\t"
    "v_rcp_f32 v142, v142\n\t"
    "v_rcp_f32 v143, v143\n\t"
    "v_pk_mul_f32 v[136:137], v[136:137], v[160:161]\n\t"
    "v_pk_mul_f32 v[138:139], v[138:139], v[162:163]\n\t"
    "v_pk_mul_f32 v[140:141], v[140:141], v[164:165]\n\t"
    "v_pk_mul_f32 v[142:143], v[142:143], v[166:167]\n\t"
    "v_pk_mul_f32 v[136:137], v[136:137], v[176:177]\n\t"
    "v_pk_mul_f32 v[138:139], v[138:139], v[178:179]\n\t"
    "v_pk_mul_f32 v[140:141], v[140:141], v[180:181]\n\t"
    "v_pk_mul_f32 v[142:143], v[142:143], v[182:183]\n\t"
    "v_cvt_pk_bf16_f32 v144, v136, v137\n\t"
    "v_cvt_pk_bf16_f32 v146, v140, v141\n\t"
    "v_cvt_pk_bf16_f32 v145, v138, v139\n\t"
    "v_cvt_pk_bf16_f32 v147, v142, v143\n\t"
    "s_add_u32 s47, s77, 0\n\t"
    "v_permlane32_swap_b32 v144, v146\n\t"
    "v_permlane32_swap_b32 v145, v147\n\t"
    "s_nop 0\n\t"
    "buffer_store_dwordx4 v[144:147], v19, s[16:19], s47 offen sc1\n\t"
    "buffer_load_dwordx4 v17, s[12:15], s59 offen offset:2048 lds\n\t"
    "buffer_load_dwordx4 v18, s[12:15], s59 offen offset:3072 lds\n\t"
    "v_pk_mul_f32 v[148:149], v[168:169], s[28:29]\n\t"
    "v_pk_mul_f32 v[150:151], v[170:171], s[28:29]\n\t"
    "v_pk_mul_f32 v[152:153], v[172:173], s[28:29]\n\t"
    "v_pk_mul_f32 v[154:155], v[174:175], s[28:29]\n\t"
    "v_exp_f32 v148, v148\n\t"
    "v_exp_f32 v149, v149\n\t"
    "v_exp_f32 v150, v150\n\t"
    "v_exp_f32 v151, v151\n\t"
    "v_exp_f32 v152, v152\n\t"
    "v_exp_f32 v153, v153\n\t"
    "v_exp_f32 v154, v154\n\t"
    "v_exp_f32 v155, v155\n\t"
    "v_pk_add_f32 v[148:149], v[148:149], 1.0 op_sel_hi:[1,0]\n\t"
    "v_pk_add_f32 v[150:151], v[150:151], 1.0 op_sel_hi:[1,0]\n\t"
    "v_pk_add_f32 v[152:153], v[152:153], 1.0 op_sel_hi:[1,0]\n\t"
    "v_pk_add_f32 v[154:155], v[154:155], 1.0 op_sel_hi:[1,0]\n\t"
    "v_rcp_f32 v148, v148\n\t"
    "v_rcp_f32 v149, v149\n\t"
    "v_rcp_f32 v150, v150\n\t"
    "v_rcp_f32 v151, v151\n\t"
    "v_rcp_f32 v152, v152\n\t"
    "v_rcp_f32 v153, v153\n\t"
    "v_rcp_f32 v154, v154\n\t"
    "v_rcp_f32 v155, v155\n\t"
    "v_pk_mul_f32 v[148:149], v[148:149], v[168:169]\n\t"
    "v_pk_mul_f32 v[150:151], v[150:151], v[170:171]\n\t"
    "v_pk_mul_f32 v[152:153], v[152:153], v[172:173]\n\t"
    "v_pk_mul_f32 v[154:155], v[154:155], v[174:175]\n\t"
    "v_pk_mul_f32 v[148:149], v[148:149], v[184:185]\n\t"
    "v_pk_mul_f32 v[150:151], v[150:151], v[186:187]\n\t"
    "v_pk_mul_f32 v[152:153], v[152:153], v[188:189]\n\t"
    "v_pk_mul_f32 v[154:155], v[154:155], v[190:191]\n\t"
    "v_cvt_pk_bf16_f32 v156, v148, v149\n\t"
    "v_cvt_pk_bf16_f32 v158, v152, v153\n\t"
    "v_cvt_pk_bf16_f32 v157, v150, v151\n\t"
    "v_cvt_pk_bf16_f32 v159, v154, v155\n\t"
    "s_add_u32 s47, s77, 88064\n\t"
    "v_permlane32_swap_b32 v156, v158\n\t"
    "v_permlane32_swap_b32 v157, v159\n\t"
    "buffer_store_dwordx4 v[156:159], v19, s[16:19], s47 offen sc1\n\t"
    "s_add_u32 m0, s42, 65536\n\t"
    "s_nop 0\n\t"
    "buffer_load_dwordx4 v15, s[12:15], s60 offen offset:0 lds\n\t"
    "buffer_load_dwordx4 v16, s[12:15], s60 offen offset:1024 lds\n\t"
    "s_cmp_gt_u32 s36, 1\n\t"
    "s_cbranch_scc0 L_skip2%=\n\t"
    "s_nop 0\n\t"
    "v_pk_mul_f32 v[136:137], v[192:193], s[28:29]\n\t"
    "v_pk_mul_f32 v[138:139], v[194:195], s[28:29]\n\t"
    "v_pk_mul_f32 v[140:141], v[196:197], s[28:29]\n\t"
    "v_pk_mul_f32 v[142:143], v[198:199], s[28:29]\n\t"
    "v_exp_f32 v136, v136\n\t"
    "v_exp_f32 v137, v137\n\t"
    "v_exp_f32 v138, v138\n\t"
    "v_exp_f32 v139, v139\n\t"
    "v_exp_f32 v140, v140\n\t"
    "v_exp_f32 v141, v141\n\t"
    "v_exp_f32 v142, v142\n\t"
    "v_exp_f32 v143, v143\n\t"
    "v_pk_add_f32 v[136:137], v[136:137], 1.0 op_sel_hi:[1,0]\n\t"
    "v_pk_add_f32 v[138:139], v[138:139], 1.0 op_sel_hi:[1,0]\n\t"
    "v_pk_add_f32 v[140:141], v[140:141], 1.0 op_sel_hi:[1,0]\n\t"
    "v_pk_add_f32 v[142:143], v[142:143], 1.0 op_sel_hi:[1,0]\n\t"
    "v_rcp_f32 v136, v136\n\t"
    "v_rcp_f32 v137, v137\n\t"
    "v_rcp_f32 v138, v138\n\t"
    "v_rcp_f32 v139, v139\n\t"
    "v_rcp_f32 v140, v140\n\t"
    "v_rcp_f32 v141, v141\n\t"
    "v_rcp_f32 v142, v142\n\t"
    "v_rcp_f32 v143, v143\n\t"
    "v_pk_mul_f32 v[136:137], v[136:137], v[192:193]\n\t"
    "v_pk_mul_f32 v[138:139], v[138:139], v[194:195]\n\t"
    "v_pk_mul_f32 v[140:141], v[140:141], v[196:197]\n\t"
    "v_pk_mul_f32 v[142:143], v[142:143], v[198:199]\n\t"
    "v_pk_mul_f32 v[136:137], v[136:137], v[208:209]\n\t"
    "v_pk_mul_f32 v[138:139], v[138:139], v[210:211]\n\t"
    "v_pk_mul_f32 v[140:141], v[140:141], v[212:213]\n\t"
    "v_pk_mul_f32 v[142:143], v[142:143], v[214:215]\n\t"
    "v_cvt_pk_bf16_f32 v144, v136, v137\n\t"
    "v_cvt_pk_bf16_f32 v146, v140, v141\n\t"
    "v_cvt_pk_bf16_f32 v145, v138, v139\n\t"
    "v_cvt_pk_bf16_f32 v147, v142, v143\n\t"
    "s_add_u32 s47, s52, 16384\n\t"
    "v_permlane32_swap_b32 v144, v146\n\t"
    "v_permlane32_swap_b32 v145, v147\n\t"
    "buffer_store_dwordx4 v[144:147], v19, s[16:19], s47 offen sc1\n\t"
    "L_skip2%=:\n\t"
    "buffer_load_dwordx4 v17, s[12:15], s60 offen offset:2048 lds\n\t"
    "buffer_load_dwordx4 v18, s[12:15], s60 offen offset:3072 lds\n\t"
    "s_cmp_gt_u32 s36, 1\n\t"
    "s_cbranch_scc0 L_skip3%=\n\t"
    "v_pk_mul_f32 v[148:149], v[200:201], s[28:29]\n\t"
    "v_pk_mul_f32 v[150:151], v[202:203], s[28:29]\n\t"
    "v_pk_mul_f32 v[152:153], v[204:205], s[28:29]\n\t"
    "v_pk_mul_f32 v[154:155], v[206:207], s[28:29]\n\t"
    "v_exp_f32 v148, v148\n\t"
    "v_exp_f32 v149, v149\n\t"
    "v_exp_f32 v150, v150\n\t"
    "v_exp_f32 v151, v151\n\t"
    "v_exp_f32 v152, v152\n\t"
    "v_exp_f32 v153, v153\n\t"
    "v_exp_f32 v154, v154\n\t"
    "v_exp_f32 v155, v155\n\t"
    "v_pk_add_f32 v[148:149], v[148:149], 1.0 op_sel_hi:[1,0]\n\t"
    "v_pk_add_f32 v[150:151], v[150:151], 1.0 op_sel_hi:[1,0]\n\t"
    "v_pk_add_f32 v[152:153], v[152:153], 1.0 op_sel_hi:[1,0]\n\t"
    "v_pk_add_f32 v[154:155], v[154:155], 1.0 op_sel_hi:[1,0]\n\t"
    "v_rcp_f32 v148, v148\n\t"
    "v_rcp_f32 v149, v149\n\t"
    "v_rcp_f32 v150, v150\n\t"
    "v_rcp_f32 v151, v151\n\t"
    "v_rcp_f32 v152, v152\n\t"
    "v_rcp_f32 v153, v153\n\t"
    "v_rcp_f32 v154, v154\n\t"
    "v_rcp_f32 v155, v155\n\t"
    "v_pk_mul_f32 v[148:149], v[148:149], v[200:201]\n\t"
    "v_pk_mul_f32 v[150:151], v[150:151], v[202:203]\n\t"
    "v_pk_mul_f32 v[152:153], v[152:153], v[204:205]\n\t"
    "v_pk_mul_f32 v[154:155], v[154:155], v[206:207]\n\t"
    "v_pk_mul_f32 v[148:149], v[148:149], v[216:217]\n\t"
    "v_pk_mul_f32 v[150:151], v[150:151], v[218:219]\n\t"
    "v_pk_mul_f32 v[152:153], v[152:153], v[220:221]\n\t"
    "v_pk_mul_f32 v[154:155], v[154:155], v[222:223]\n\t"
    "v_cvt_pk_bf16_f32 v156, v148, v149\n\t"
    "v_cvt_pk_bf16_f32 v158, v152, v153\n\t"
    "v_cvt_pk_bf16_f32 v157, v150, v151\n\t"
    "v_cvt_pk_bf16_f32 v159, v154, v155\n\t"
    "s_add_u32 s47, s52, 104448\n\t"
    "v_permlane32_swap_b32 v156, v158\n\t"
    "v_permlane32_swap_b32 v157, v159\n\t"
    "buffer_store_dwordx4 v[156:159], v19, s[16:19], s47 offen sc1\n\t"
    "L_skip3%=:\n\t"
    "s_add_u32 m0, s43, 81920\n\t"
    "s_nop 0\n\t"
    "buffer_load_dwordx4 v15, s[12:15], s61 offen offset:0 lds\n\t"
    "s_cmp_gt_u32 s36, 2\n\t"
    "s_cbranch_scc0 L_skip4%=\n\t"
    "s_nop 0\n\t"
    "v_pk_mul_f32 v[136:137], v[224:225], s[28:29]\n\t"
    "v_pk_mul_f32 v[138:139], v[226:227], s[28:29]\n\t"
    "v_pk_mul_f32 v[140:141], v[228:229], s[28:29]\n\t"
    "v_pk_mul_f32 v[142:143], v[230:231], s[28:29]\n\t"
    "v_exp_f32 v136, v136\n\t"
    "v_exp_f32 v137, v137\n\t"
    "v_exp_f32 v138, v138\n\t"
    "v_exp_f32 v139, v139\n\t"
    "v_exp_f32 v140, v140\n\t"
    "v_exp_f32 v141, v141\n\t"
    "v_exp_f32 v142, v142\n\t"
    "v_exp_f32 v143, v143\n\t"
    "v_pk_add_f32 v[136:137], v[136:137], 1.0 op_sel_hi:[1,0]\n\t"
    "v_pk_add_f32 v[138:139], v[138:139], 1.0 op_sel_hi:[1,0]\n\t"
    "v_pk_add_f32 v[140:141], v[140:141], 1.0 op_sel_hi:[1,0]\n\t"
    "v_pk_add_f32 v[142:143], v[142:143], 1.0 op_sel_hi:[1,0]\n\t"
    "v_rcp_f32 v136, v136\n\t"
    "v_rcp_f32 v137, v137\n\t"
    "v_rcp_f32 v138, v138\n\t"
    "v_rcp_f32 v139, v139\n\t"
    "v_rcp_f32 v140, v140\n\t"
    "v_rcp_f32 v141, v141\n\t"
    "v_rcp_f32 v142, v142\n\t"
    "v_rcp_f32 v143, v143\n\t"
    "v_pk_mul_f32 v[136:137], v[136:137], v[224:225]\n\t"
    "v_pk_mul_f32 v[138:139], v[138:139], v[226:227]\n\t"
    "v_pk_mul_f32 v[140:141], v[140:141], v[228:229]\n\t"
    "v_pk_mul_f32 v[142:143], v[142:143], v[230:231]\n\t"
    "v_pk_mul_f32 v[136:137], v[136:137], v[240:241]\n\t"
    "v_pk_mul_f32 v[138:139], v[138:139], v[242:243]\n\t"
    "v_pk_mul_f32 v[140:141], v[140:141], v[244:245]\n\t"
    "v_pk_mul_f32 v[142:143], v[142:143], v[246:247]\n\t"
    "v_cvt_pk_bf16_f32 v144, v136, v137\n\t"
    "v_cvt_pk_bf16_f32 v146, v140, v141\n\t"
    "v_cvt_pk_bf16_f32 v145, v138, v139\n\t"
    "v_cvt_pk_bf16_f32 v147, v142, v143\n\t"
    "s_add_u32 s47, s52, 17408\n\t"
    "v_permlane32_swap_b32 v144, v146\n\t"
    "v_permlane32_swap_b32 v145, v147\n\t"
    "buffer_store_dwordx4 v[144:147], v19, s[16:19], s47 offen sc1\n\t"
    "L_skip4%=:\n\t"
    "buffer_load_dwordx4 v16, s[12:15], s61 offen offset:1024 lds\n\t"
    "s_cmp_gt_u32 s36, 2\n\t"
    "s_cbranch_scc0 L_skip5%=\n\t"
    "v_pk_mul_f32 v[148:149], v[232:233], s[28:29]\n\t"
    "v_pk_mul_f32 v[150:151], v[234:235], s[28:29]\n\t"
    "v_pk_mul_f32 v[152:153], v[236:237], s[28:29]\n\t"
    "v_pk_mul_f32 v[154:155], v[238:239], s[28:29]\n\t"
    "v_exp_f32 v148, v148\n\t"
    "v_exp_f32 v149, v149\n\t"
    "v_exp_f32 v150, v150\n\t"
    "v_exp_f32 v151, v151\n\t"
    "v_exp_f32 v152, v152\n\t"
    "v_exp_f32 v153, v153\n\t"
    "v_exp_f32 v154, v154\n\t"
    "v_exp_f32 v155, v155\n\t"
    "v_pk_add_f32 v[148:149], v[148:149], 1.0 op_sel_hi:[1,0]\n\t"
    "v_pk_add_f32 v[150:151], v[150:151], 1.0 op_sel_hi:[1,0]\n\t"
    "v_pk_add_f32 v[152:153], v[152:153], 1.0 op_sel_hi:[1,0]\n\t"
    "v_pk_add_f32 v[154:155], v[154:155], 1.0 op_sel_hi:[1,0]\n\t"
    "v_rcp_f32 v148, v148\n\t"
    "v_rcp_f32 v149, v149\n\t"
    "v_rcp_f32 v150, v150\n\t"
    "v_rcp_f32 v151, v151\n\t"
    "v_rcp_f32 v152, v152\n\t"
    "v_rcp_f32 v153, v153\n\t"
    "v_rcp_f32 v154, v154\n\t"
    "v_rcp_f32 v155, v155\n\t"
    "v_pk_mul_f32 v[148:149], v[148:149], v[232:233]\n\t"
    "v_pk_mul_f32 v[150:151], v[150:151], v[234:235]\n\t"
    "v_pk_mul_f32 v[152:153], v[152:153], v[236:237]\n\t"
    "v_pk_mul_f32 v[154:155], v[154:155], v[238:239]\n\t"
    "v_pk_mul_f32 v[148:149], v[148:149], v[248:249]\n\t"
    "v_pk_mul_f32 v[150:151], v[150:151], v[250:251]\n\t"
    "v_pk_mul_f32 v[152:153], v[152:153], v[252:253]\n\t"
    "v_pk_mul_f32 v[154:155], v[154:155], v[254:255]\n\t"
    "v_cvt_pk_bf16_f32 v156, v148, v149\n\t"
    "v_cvt_pk_bf16_f32 v158, v152, v153\n\t"
    "v_cvt_pk_bf16_f32 v157, v150, v151\n\t"
    "v_cvt_pk_bf16_f32 v159, v154, v155\n\t"
    "s_add_u32 s47, s52, 105472\n\t"
    "v_permlane32_swap_b32 v156, v158\n\t"
    "v_permlane32_swap_b32 v157, v159\n\t"
    "buffer_store_dwordx4 v[156:159], v19, s[16:19], s47 offen sc1\n\t"
    "s_waitcnt vmcnt(10)\n\t"
    "s_branch L_fa%=\n\t"
    "L_skip5%=:\n\t"
    "s_waitcnt vmcnt(6)\n\t"
    "L_fa%=:\n\t"
    "s_mov_b32 exec_lo, 0\n\t"
    "s_mov_b32 exec_hi, 0xff\n\t"
    "buffer_store_dwordx2 v[24:25], v20, s[16:19], 0 offen offset:0 sc1\n\t"
    "s_mov_b64 exec, -1\n\t"
    "s_barrier\n\t"
    "s_add_u32 m0, s42, 16384\n\t"
    "s_nop 0\n\t"
    "buffer_load_dwordx4 v15, s[12:15], s62 offen offset:0 lds\n\t"
    "buffer_load_dwordx4 v16, s[12:15], s62 offen offset:1024 lds\n\t"
    "ds_read_b64_tr_b16 a[0:1], v36 offset:49152\n\t"
    "ds_read_b64_tr_b16 a[2:3], v36 offset:50176\n\t"
    "ds_read_b64_tr_b16 a[4:5], v37 offset:49152\n\t"
    "ds_read_b64_tr_b16 a[6:7], v37 offset:50176\n\t"
    "ds_read_b64_tr_b16 a[8:9], v38 offset:49152\n\t"
    "ds_read_b64_tr_b16 a[10:11], v38 offset:50176\n\t"
    "ds_read_b64_tr_b16 a[12:13], v39 offset:49152\n\t"
    "ds_read_b64_tr_b16 a[14:15], v39 offset:50176\n\t"
    "buffer_load_dwordx4 v17, s[12:15], s62 offen offset:2048 lds\n\t"
    "ds_read_b64_tr_b16 a[16:17], v36 offset:53248\n\t"
    "ds_read_b64_tr_b16 a[18:19], v36 offset:54272\n\t"
    "ds_read_b64_tr_b16 a[20:21], v37 offset:53248\n\t"
    "ds_read_b64_tr_b16 a[22:23], v37 offset:54272\n\t"
    "ds_read_b64_tr_b16 a[24:25], v38 offset:53248\n\t"
    "ds_read_b64_tr_b16 a[26:27], v38 offset:54272\n\t"
    "ds_read_b64_tr_b16 a[28:29], v39 offset:53248\n\t"
    "ds_read_b64_tr_b16 a[30:31], v39 offset:54272\n\t"
    "buffer_load_dwordx4 v18, s[12:15], s62 offen offset:3072 lds\n\t"
    "ds_read_b64_tr_b16 a[32:33], v36 offset:57344\n\t"
    "ds_read_b64_tr_b16 a[34:35], v36 offset:58368\n\t"
    "ds_read_b64_tr_b16 a[36:37], v37 offset:57344\n\t"
    "ds_read_b64_tr_b16 a[38:39], v37 offset:58368\n\t"
    "ds_read_b64_tr_b16 a[40:41], v38 offset:57344\n\t"
    "ds_read_b64_tr_b16 a[42:43], v38 offset:58368\n\t"
    "ds_read_b64_tr_b16 a[44:45], v39 offset:57344\n\t"
    "ds_read_b64_tr_b16 a[46:47], v39 offset:58368\n\t"
    "s_add_u32 m0, s42, 90112\n\t"
    "s_nop 0\n\t"
    "buffer_load_dwordx4 v15, s[12:15], s63 offen offset:0 lds\n\t"
    "s_cmp_eq_u32 s30, 0\n\t"
    "s_cbranch_scc0 L_nop0%=\n\t"
    "s_mov_b64 exec, 0xffffffff\n\t"
    "buffer_atomic_add_x2 v[54:55], v21, s[16:19], 0 offen offset:0 sc0\n\t"
    "s_mov_b64 exec, -1\n\t"
    "L_nop0%=:\n\t"
    "ds_read_b64_tr_b16 a[48:49], v36 offset:61440\n\t"
    "ds_read_b64_tr_b16 a[50:51], v36 offset:62464\n\t"
    "ds_read_b64_tr_b16 a[52:53], v37 offset:61440\n\t"
    "ds_read_b64_tr_b16 a[54:55], v37 offset:62464\n\t"
    "ds_read_b64_tr_b16 a[56:57], v38 offset:61440\n\t"
    "ds_read_b64_tr_b16 a[58:59], v38 offset:62464\n\t"
    "ds_read_b64_tr_b16 a[60:61], v39 offset:61440\n\t"
    "ds_read_b64_tr_b16 a[62:63], v39 offset:62464\n\t"
    "buffer_load_dwordx4 v16, s[12:15], s63 offen offset:1024 lds\n\t"
    "ds_read_b64_tr_b16 a[64:65], v44 offset:8192\n\t"
    "ds_read_b64_tr_b16 a[66:67], v44 offset:9216\n\t"
    "ds_read_b64_tr_b16 a[68:69], v45 offset:8192\n\t"
    "ds_read_b64_tr_b16 a[70:71], v45 offset:9216\n\t"
    "ds_read_b64_tr_b16 a[72:73], v46 offset:8192\n\t"
    "ds_read_b64_tr_b16 a[74:75], v46 offset:9216\n\t"
    "ds_read_b64_tr_b16 a[76:77], v47 offset:8192\n\t"
    "ds_read_b64_tr_b16 a[78:79], v47 offset:9216\n\t"
    "buffer_load_dwordx4 v17, s[12:15], s63 offen offset:2048 lds\n\t"
    "ds_read_b64_tr_b16 a[80:81], v44 offset:12288\n\t"
    "ds_read_b64_tr_b16 a[82:83], v44 offset:13312\n\t"
    "ds_read_b64_tr_b16 a[84:85], v45 offset:12288\n\t"
    "ds_read_b64_tr_b16 a[86:87], v45 offset:13312\n\t"
    "ds_read_b64_tr_b16 a[88:89], v46 offset:12288\n\t"
    "ds_read_b64_tr_b16 a[90:91], v46 offset:13312\n\t"
    "ds_read_b64_tr_b16 a[92:93], v47 offset:12288\n\t"
    "ds_read_b64_tr_b16 a[94:95], v47 offset:13312\n\t"
    "ds_read_b64_tr_b16 a[96:97], v44 offset:16384\n\t"
    "ds_read_b64_tr_b16 a[98:99], v44 offset:17408\n\t"
    "ds_read_b64_tr_b16 a[100:101], v45 offset:16384\n\t"
    "ds_read_b64_tr_b16 a[102:103], v45 offset:17408\n\t"
    "ds_read_b64_tr_b16 a[104:105], v46 offset:16384\n\t"
    "ds_read_b64_tr_b16 a[106:107], v46 offset:17408\n\t"
    "ds_read_b64_tr_b16 a[108:109], v47 offset:16384\n\t"
    "ds_read_b64_tr_b16 a[110:111], v47 offset:17408\n\t"
    "ds_read_b64_tr_b16 a[112:113], v44 offset:20480\n\t"
    "ds_read_b64_tr_b16 a[114:115], v44 offset:21504\n\t"
    "ds_read_b64_tr_b16 a[116:117], v45 offset:20480\n\t"
    "ds_read_b64_tr_b16 a[118:119], v45 offset:21504\n\t"
    "ds_read_b64_tr_b16 a[120:121], v46 offset:20480\n\t"
    "ds_read_b64_tr_b16 a[122:123], v46 offset:21504\n\t"
    "ds_read_b64_tr_b16 a[124:125], v47 offset:20480\n\t"
    "ds_read_b64_tr_b16 a[126:127], v47 offset:21504\n\t"
    "s_cmp_eq_u32 s30, 0\n\t"
    "s_cbranch_scc0 L_nop1%=\n\t"
    "s_mov_b64 exec, 0xffffffff\n\t"
    "buffer_atomic_add_x2 v[56:57], v21, s[16:19], 0 offen offset:0 sc0\n\t"
    "s_mov_b64 exec, -1\n\t"
    "s_waitcnt vmcnt(9)\n\t"
    "s_branch L_nopx1%=\n\t"
    "L_nop1%=:\n\t"
    "s_waitcnt vmcnt(7)\n\t"
    "L_nopx1%=:\n\t"
    "s_mov_b32 exec_lo, 0\n\t"
    "s_mov_b32 exec_hi, 0xff\n\t"
    "buffer_store_dwordx2 v[24:25], v20, s[16:19], 0 offen offset:256 sc1\n\t"
    "s_mov_b64 exec, -1\n\t"
    "s_cmp_eq_u32 s30, 0\n\t"
    "s_cbranch_scc0 L_nop2%=\n\t"
    "s_mov_b64 exec, 0xffffffff\n\t"
    "buffer_atomic_add_x2 v[58:59], v21, s[16:19], 0 offen offset:0 sc0\n\t"
    "s_mov_b64 exec, -1\n\t"
    "L_nop2%=:\n\t"
    "buffer_load_dwordx4 v18, s[12:15], s63 offen offset:3072 lds\n\t"
    "s_add_u32 m0, s43, 106496\n\t"
    "s_nop 0\n\t"
    "buffer_load_dwordx4 v15, s[12:15], s64 offen offset:0 lds\n\t"
    "ds_read_b64_tr_b16 a[128:129], v44 offset:24576\n\t"
    "ds_read_b64_tr_b16 a[130:131], v44 offset:25600\n\t"
    "ds_read_b64_tr_b16 a[132:133], v45 offset:24576\n\t"
    "ds_read_b64_tr_b16 a[134:135], v45 offset:25600\n\t"
    "ds_read_b64_tr_b16 a[136:137], v46 offset:24576\n\t"
    "ds_read_b64_tr_b16 a[138:139], v46 offset:25600\n\t"
    "ds_read_b64_tr_b16 a[140:141], v47 offset:24576\n\t"
    "ds_read_b64_tr_b16 a[142:143], v47 offset:25600\n\t"
    "buffer_load_dwordx4 v16, s[12:15], s64 offen offset:1024 lds\n\t"
    "ds_read_b64_tr_b16 a[144:145], v44 offset:28672\n\t"
    "ds_read_b64_tr_b16 a[146:147], v44 offset:29696\n\t"
    "ds_read_b64_tr_b16 a[148:149], v45 offset:28672\n\t"
    "ds_read_b64_tr_b16 a[150:151], v45 offset:29696\n\t"
    "ds_read_b64_tr_b16 a[152:153], v46 offset:28672\n\t"
    "ds_read_b64_tr_b16 a[154:155], v46 offset:29696\n\t"
    "ds_read_b64_tr_b16 a[156:157], v47 offset:28672\n\t"
    "ds_read_b64_tr_b16 a[158:159], v47 offset:29696\n\t"
    "s_cmp_eq_u32 s30, 0\n\t"
    "s_cbranch_scc0 L_polled%=\n\t"
    "s_mov_b64 exec, 0xffffffff\n\t"
    "s_waitcnt vmcnt(8)\n\t"
    "v_cmp_ne_u64 vcc, s[80:81], v[54:55]\n\t"
    "v_mov_b32 v54, 0\n\t"
    "v_mov_b32 v55, 0\n\t"
    "s_nop 2\n\t"
    "s_cbranch_vccz L_polled%=\n\t"
    "buffer_atomic_add_x2 v[54:55], v21, s[16:19], 0 offen offset:0 sc0\n\t"
    "s_waitcnt vmcnt(6)\n\t"
    "v_cmp_ne_u64 vcc, s[80:81], v[56:57]\n\t"
    "v_mov_b32 v56, 0\n\t"
    "v_mov_b32 v57, 0\n\t"
    "s_nop 2\n\t"
    "s_cbranch_vccz L_polled%=\n\t"
    "buffer_atomic_add_x2 v[56:57], v21, s[16:19], 0 offen offset:0 sc0\n\t"
    "s_waitcnt vmcnt(5)\n\t"
    "v_cmp_ne_u64 vcc, s[80:81], v[58:59]\n\t"
    "v_mov_b32 v58, 0\n\t"
    "v_mov_b32 v59, 0\n\t"
    "s_nop 2\n\t"
    "s_cbranch_vccz L_polled%=\n\t"
    "buffer_atomic_add_x2 v[58:59], v21, s[16:19], 0 offen offset:0 sc0\n\t"
    "L_poll%=:\n\t"
    "s_waitcnt vmcnt(2)\n\t"
    "v_cmp_ne_u64 vcc, s[80:81], v[54:55]\n\t"
    "v_mov_b32 v54, 0\n\t"
    "v_mov_b32 v55, 0\n\t"
    "s_nop 2\n\t"
    "s_cbranch_vccz L_polled%=\n\t"
    "buffer_atomic_add_x2 v[54:55], v21, s[16:19], 0 offen offset:0 sc0\n\t"
    "s_waitcnt vmcnt(2)\n\t"
    "v_cmp_ne_u64 vcc, s[80:81], v[56:57]\n\t"
    "v_mov_b32 v56, 0\n\t"
    "v_mov_b32 v57, 0\n\t"
    "s_nop 2\n\t"
    "s_cbranch_vccz L_polled%=\n\t"
    "buffer_atomic_add_x2 v[56:57], v21, s[16:19], 0 offen offset:0 sc0\n\t"
    "s_waitcnt vmcnt(2)\n\t"
    "v_cmp_ne_u64 vcc, s[80:81], v[58:59]\n\t"
    "v_mov_b32 v58, 0\n\t"
    "v_mov_b32 v59, 0\n\t"
    "s_nop 2\n\t"
    "s_cbranch_vccz L_polled%=\n\t"
    "buffer_atomic_add_x2 v[58:59], v21, s[16:19], 0 offen offset:0 sc0\n\t"
    "s_sub_u32 s92, s92, 1\n\t"
    "s_cbranch_scc0 L_poll%=\n\t"
    "s_mov_b32 s92, 0\n\t"
    "L_polled%=:\n\t"
    "s_mov_b64 vcc, 0\n\t"
    "s_mov_b64 exec, -1\n\t"
    "s_waitcnt lgkmcnt(0)\n\t"
    "s_barrier\n\t"
    "buffer_load_dwordx4 v[160:163], v22, s[24:27], 0 offen offset:0 sc1\n\t"
    "buffer_load_dwordx4 v[164:167], v22, s[24:27], 0 offen offset:1024 sc1\n\t"
    "ds_read_b64_tr_b16 a[160:161], v36 offset:32768\n\t"
    "ds_read_b64_tr_b16 a[162:163], v36 offset:33792\n\t"
    "ds_read_b64_tr_b16 a[164:165], v37 offset:32768\n\t"
    "ds_read_b64_tr_b16 a[166:167], v37 offset:33792\n\t"
    "ds_read_b64_tr_b16 a[168:169], v38 offset:32768\n\t"
    "ds_read_b64_tr_b16 a[170:171], v38 offset:33792\n\t"
    "ds_read_b64_tr_b16 a[172:173], v39 offset:32768\n\t"
    "ds_read_b64_tr_b16 a[174:175], v39 offset:33792\n\t"
    "buffer_load_dwordx4 v[168:171], v22, s[24:27], 0 offen offset:2048 sc1\n\t"
    "ds_read_b64_tr_b16 a[176:177], v36 offset:36864\n\t"
    "ds_read_b64_tr_b16 a[178:179], v36 offset:37888\n\t"
    "ds_read_b64_tr_b16 a[180:181], v37 offset:36864\n\t"
    "ds_read_b64_tr_b16 a[182:183], v37 offset:37888\n\t"
    "ds_read_b64_tr_b16 a[184:185], v38 offset:36864\n\t"
    "ds_read_b64_tr_b16 a[186:187], v38 offset:37888\n\t"
    "ds_read_b64_tr_b16 a[188:189], v39 offset:36864\n\t"
    "ds_read_b64_tr_b16 a[190:191], v39 offset:37888\n\t"
    "buffer_load_dwordx4 v[172:175], v22, s[24:27], 0 offen offset:3072 sc1\n\t"
    "ds_read_b64_tr_b16 a[192:193], v36 offset:40960\n\t"
    "ds_read_b64_tr_b16 a[194:195], v36 offset:41984\n\t"
    "ds_read_b64_tr_b16 a[196:197], v37 offset:40960\n\t"
    "ds_read_b64_tr_b16 a[198:199], v37 offset:41984\n\t"
    "ds_read_b64_tr_b16 a[200:201], v38 offset:40960\n\t"
    "ds_read_b64_tr_b16 a[202:203], v38 offset:41984\n\t"
    "ds_read_b64_tr_b16 a[204:205], v39 offset:40960\n\t"
    "ds_read_b64_tr_b16 a[206:207], v39 offset:41984\n\t"
    "buffer_load_dwordx4 v[176:179], v22, s[24:27], s67 offen offset:0 sc1\n\t"
    "ds_read_b64_tr_b16 a[208:209], v36 offset:45056\n\t"
    "ds_read_b64_tr_b16 a[210:211], v36 offset:46080\n\t"
    "ds_read_b64_tr_b16 a[212:213], v37 offset:45056\n\t"
    "ds_read_b64_tr_b16 a[214:215], v37 offset:46080\n\t"
    "ds_read_b64_tr_b16 a[216:217], v38 offset:45056\n\t"
    "ds_read_b64_tr_b16 a[218:219], v38 offset:46080\n\t"
    "ds_read_b64_tr_b16 a[220:221], v39 offset:45056\n\t"
    "ds_read_b64_tr_b16 a[222:223], v39 offset:46080\n\t"
    "buffer_load_dwordx4 v[180:183], v22, s[24:27], s67 offen offset:1024 sc1\n\t"
    "ds_read_b64_tr_b16 a[224:225], v40 offset:49152\n\t"
    "ds_read_b64_tr_b16 a[226:227], v40 offset:50176\n\t"
    "ds_read_b64_tr_b16 a[228:229], v41 offset:49152\n\t"
    "ds_read_b64_tr_b16 a[230:231], v41 offset:50176\n\t"
    "ds_read_b64_tr_b16 a[232:233], v42 offset:49152\n\t"
    "ds_read_b64_tr_b16 a[234:235], v42 offset:50176\n\t"
    "ds_read_b64_tr_b16 a[236:237], v43 offset:49152\n\t"
    "ds_read_b64_tr_b16 a[238:239], v43 offset:50176\n\t"
    "buffer_load_dwordx4 v[184:187], v22, s[24:27], s67 offen offset:2048 sc1\n\t"
    "ds_read_b64_tr_b16 a[240:241], v40 offset:53248\n\t"
    "ds_read_b64_tr_b16 a[242:243], v40 offset:54272\n\t"
    "ds_read_b64_tr_b16 a[244:245], v41 offset:53248\n\t"
    "ds_read_b64_tr_b16 a[246:247], v41 offset:54272\n\t"
    "ds_read_b64_tr_b16 a[248:249], v42 offset:53248\n\t"
    "ds_read_b64_tr_b16 a[250:251], v42 offset:54272\n\t"
    "ds_read_b64_tr_b16 a[252:253], v43 offset:53248\n\t"
    "ds_read_b64_tr_b16 a[254:255], v43 offset:54272\n\t"
    "buffer_load_dwordx4 v[188:191], v22, s[24:27], s67 offen offset:3072 sc1\n\t"
    "ds_read_b64_tr_b16 v[80:81], v40 offset:57344\n\t"
    "ds_read_b64_tr_b16 v[82:83], v40 offset:58368\n\t"
    "ds_read_b64_tr_b16 v[84:85], v41 offset:57344\n\t"
    "ds_read_b64_tr_b16 v[86:87], v41 offset:58368\n\t"
    "ds_read_b64_tr_b16 v[88:89], v42 offset:57344\n\t"
    "ds_read_b64_tr_b16 v[90:91], v42 offset:58368\n\t"
    "ds_read_b64_tr_b16 v[92:93], v43 offset:57344\n\t"
    "ds_read_b64_tr_b16 v[94:95], v43 offset:58368\n\t"
    "buffer_load_dwordx4 v[192:195], v22, s[24:27], s68 offen offset:0 sc1\n\t"
    "ds_read_b64_tr_b16 v[96:97], v40 offset:61440\n\t"
    "ds_read_b64_tr_b16 v[98:99], v40 offset:62464\n\t"
    "ds_read_b64_tr_b16 v[100:101], v41 offset:61440\n\t"
    "ds_read_b64_tr_b16 v[102:103], v41 offset:62464\n\t"
    "ds_read_b64_tr_b16 v[104:105], v42 offset:61440\n\t"
    "ds_read_b64_tr_b16 v[106:107], v42 offset:62464\n\t"
    "ds_read_b64_tr_b16 v[108:109], v43 offset:61440\n\t"
    "ds_read_b64_tr_b16 v[110:111], v43 offset:62464\n\t"
    "buffer_load_dwordx2 v[26:27], v21, s[16:19], 0 offen offset:256 sc1\n\t"
    "buffer_load_dwordx4 v[196:199], v22, s[24:27], s68 offen offset:1024 sc1\n\t"
    "ds_read_b64_tr_b16 v[112:113], v44 offset:0\n\t"
    "ds_read_b64_tr_b16 v[114:115], v44 offset:1024\n\t"
    "ds_read_b64_tr_b16 v[116:117], v45 offset:0\n\t"
    "ds_read_b64_tr_b16 v[118:119], v45 offset:1024\n\t"
    "ds_read_b64_tr_b16 v[120:121], v46 offset:0\n\t"
    "ds_read_b64_tr_b16 v[122:123], v46 offset:1024\n\t"
    "ds_read_b64_tr_b16 v[124:125], v47 offset:0\n\t"
    "ds_read_b64_tr_b16 v[126:127], v47 offset:1024\n\t"
    "buffer_load_dwordx4 v[200:203], v22, s[24:27], s68 offen offset:2048 sc1\n\t"
    "ds_read_b64_tr_b16 v[128:129], v44 offset:4096\n\t"
    "ds_read_b64_tr_b16 v[130:131], v44 offset:5120\n\t"
    "ds_read_b64_tr_b16 v[132:133], v45 offset:4096\n\t"
    "ds_read_b64_tr_b16 v[134:135], v45 offset:5120\n\t"
    "ds_read_b64_tr_b16 v[136:137], v46 offset:4096\n\t"
    "ds_read_b64_tr_b16 v[138:139], v46 offset:5120\n\t"
    "ds_read_b64_tr_b16 v[140:141], v47 offset:4096\n\t"
    "ds_read_b64_tr_b16 v[142:143], v47 offset:5120\n\t"
    "ds_read_b64_tr_b16 v[144:145], v36 offset:0\n\t"
    "ds_read_b64_tr_b16 v[146:147], v36 offset:1024\n\t"
    "ds_read_b64_tr_b16 v[148:149], v37 offset:0\n\t"
    "ds_read_b64_tr_b16 v[150:151], v37 offset:1024\n\t"
    "ds_read_b64_tr_b16 v[152:153], v38 offset:0\n\t"
    "ds_read_b64_tr_b16 v[154:155], v38 offset:1024\n\t"
    "ds_read_b64_tr_b16 v[156:157], v39 offset:0\n\t"
    "ds_read_b64_tr_b16 v[158:159], v39 offset:1024\n\t"
    "s_waitcnt vmcnt(11)\n\t"
    "v_mfma_f32_16x16x32_bf16 v[60:63], a[0:3], v[160:163], 0\n\t"
    "s_barrier\n\t"
    "ds_read_b64_tr_b16 a[0:1], v36 offset:4096\n\t"
    "ds_read_b64_tr_b16 a[2:3], v36 offset:5120\n\t"
    "v_mfma_f32_16x16x32_bf16 v[64:67], a[4:7], v[160:163], 0\n\t"
    "ds_read_b64_tr_b16 a[4:5], v37 offset:4096\n\t"
    "v_mfma_f32_16x16x32_bf16 v[68:71], a[8:11], v[160:163], 0\n\t"
    "ds_read_b64_tr_b16 a[6:7], v37 offset:5120\n\t"
    "v_mfma_f32_16x16x32_bf16 v[72:75], a[12:15], v[160:163], 0\n\t"
    "buffer_load_dwordx4 v[204:207], v22, s[24:27], s68 offen offset:3072 sc1\n\t"
    "ds_read_b64_tr_b16 a[8:9], v38 offset:4096\n\t"
    "s_waitcnt vmcnt(11)\n\t"
    "v_mfma_f32_16x16x32_bf16 v[60:63], a[16:19], v[164:167], v[60:63]\n\t"
    "ds_read_b64_tr_b16 a[10:11], v38 offset:5120\n\t"
    "v_mfma_f32_16x16x32_bf16 v[64:67], a[20:23], v[164:167], v[64:67]\n\t"
    "ds_read_b64_tr_b16 a[12:13], v39 offset:4096\n\t"
    "v_mfma_f32_16x16x32_bf16 v[68:71], a[24:27], v[164:167], v[68:71]\n\t"
    "ds_read_b64_tr_b16 a[14:15], v39 offset:5120\n\t"
    "v_mfma_f32_16x16x32_bf16 v[72:75], a[28:31], v[164:167], v[72:75]\n\t"
    "buffer_load_dwordx4 v[208:211], v22, s[24:27], s69 offen offset:0 sc1\n\t"
    "ds_read_b64_tr_b16 a[16:17], v36 offset:8192\n\t"
    "s_waitcnt vmcnt(11)\n\t"
    "v_mfma_f32_16x16x32_bf16 v[60:63], a[32:35], v[168:171], v[60:63]\n\t"
    "ds_read_b64_tr_b16 a[18:19], v36 offset:9216\n\t"
    "v_mfma_f32_16x16x32_bf16 v[64:67], a[36:39], v[168:171], v[64:67]\n\t"
    "ds_read_b64_tr_b16 a[20:21], v37 offset:8192\n\t"
    "ds_read_b64_tr_b16 a[22:23], v37 offset:9216\n\t"
    "v_mfma_f32_16x16x32_bf16 v[68:71], a[40:43], v[168:171], v[68:71]\n\t"
    "ds_read_b64_tr_b16 a[24:25], v38 offset:8192\n\t"
    "v_mfma_f32_16x16x32_bf16 v[72:75], a[44:47], v[168:171], v[72:75]\n\t"
    "buffer_load_dwordx4 v[212:215], v22, s[24:27], s69 offen offset:1024 sc1\n\t"
    "ds_read_b64_tr_b16 a[26:27], v38 offset:9216\n\t"
    "s_waitcnt vmcnt(11)\n\t"
    "v_mfma_f32_16x16x32_bf16 v[60:63], a[48:51], v[172:175], v[60:63]\n\t"
    "ds_read_b64_tr_b16 a[28:29], v39 offset:8192\n\t"
    "v_mfma_f32_16x16x32_bf16 v[64:67], a[52:55], v[172:175], v[64:67]\n\t"
    "ds_read_b64_tr_b16 a[30:31], v39 offset:9216\n\t"
    "v_mfma_f32_16x16x32_bf16 v[68:71], a[56:59], v[172:175], v[68:71]\n\t"
    "L_hop3_%=:\n\t"
    "s_cbranch_vccnz L_hopback%=\n\t"
    "ds_read_b64_tr_b16 a[32:33], v36 offset:12288\n\t"
    "v_mfma_f32_16x16x32_bf16 v[72:75], a[60:63], v[172:175], v[72:75]\n\t"
    "buffer_load_dwordx4 v[216:219], v22, s[24:27], s69 offen offset:2048 sc1\n\t"
    "ds_read_b64_tr_b16 a[34:35], v36 offset:13312\n\t"
    "s_waitcnt vmcnt(11)\n\t"
    "v_mfma_f32_16x16x32_bf16 v[60:63], a[64:67], v[176:179], v[60:63]\n\t"
    "ds_read_b64_tr_b16 a[36:37], v37 offset:12288\n\t"
    "v_mfma_f32_16x16x32_bf16 v[64:67], a[68:71], v[176:179], v[64:67]\n\t"
    "ds_read_b64_tr_b16 a[38:39], v37 offset:13312\n\t"
    "v_mfma_f32_16x16x32_bf16 v[68:71], a[72:75], v[176:179], v[68:71]\n\t"
    "ds_read_b64_tr_b16 a[40:41], v38 offset:12288\n\t"
    "v_mfma_f32_16x16x32_bf16 v[72:75], a[76:79], v[176:179], v[72:75]\n\t"
    "buffer_load_dwordx4 v[220:223], v22, s[24:27], s69 offen offset:3072 sc1\n\t"
    "ds_read_b64_tr_b16 a[42:43], v38 offset:13312\n\t"
    "ds_read_b64_tr_b16 a[44:45], v39 offset:12288\n\t"
    "s_waitcnt vmcnt(11)\n\t"
    "v_mfma_f32_16x16x32_bf16 v[60:63], a[80:83], v[180:183], v[60:63]\n\t"
    "ds_read_b64_tr_b16 a[46:47], v39 offset:13312\n\t"
    "v_mfma_f32_16x16x32_bf16 v[64:67], a[84:87], v[180:183], v[64:67]\n\t"
    "ds_read_b64_tr_b16 a[48:49], v40 offset:0\n\t"
    "v_mfma_f32_16x16x32_bf16 v[68:71], a[88:91], v[180:183], v[68:71]\n\t"
    "s_add_u32 m0, s42, 49152\n\t"
    "ds_read_b64_tr_b16 a[50:51], v40 offset:1024\n\t"
    "v_mfma_f32_16x16x32_bf16 v[72:75], a[92:95], v[180:183], v[72:75]\n\t"
    "buffer_load_dwordx4 v15, s[12:15], s65 offen offset:0 lds\n\t"
    "ds_read_b64_tr_b16 a[52:53], v41 offset:0\n\t"
    "s_waitcnt vmcnt(11)\n\t"
    "v_mfma_f32_16x16x32_bf16 v[60:63], a[96:99], v[184:187], v[60:63]\n\t"
    "ds_read_b64_tr_b16 a[54:55], v41 offset:1024\n\t"
    "v_mfma_f32_16x16x32_bf16 v[64:67], a[100:103], v[184:187], v[64:67]\n\t"
    "ds_read_b64_tr_b16 a[56:57], v42 offset:0\n\t"
    "v_mfma_f32_16x16x32_bf16 v[68:71], a[104:107], v[184:187], v[68:71]\n\t"
    "ds_read_b64_tr_b16 a[58:59], v42 offset:1024\n\t"
    "v_mfma_f32_16x16x32_bf16 v[72:75], a[108:111], v[184:187], v[72:75]\n\t"
    "buffer_load_dwordx4 v16, s[12:15], s65 offen offset:1024 lds\n\t"
    "ds_read_b64_tr_b16 a[60:61], v43 offset:0\n\t"
    "s_waitcnt vmcnt(11)\n\t"
    "v_mfma_f32_16x16x32_bf16 v[60:63], a[112:115], v[188:191], v[60:63]\n\t"
    "ds_read_b64_tr_b16 a[62:63], v43 offset:1024\n\t"
    "v_mfma_f32_16x16x32_bf16 v[64:67], a[116:119], v[188:191], v[64:67]\n\t"
    "ds_read_b64_tr_b16 a[64:65], v40 offset:4096\n\t"
    "ds_read_b64_tr_b16 a[66:67], v40 offset:5120\n\t"
    "v_mfma_f32_16x16x32_bf16 v[68:71], a[120:123], v[188:191], v[68:71]\n\t"
    "ds_read_b64_tr_b16 a[68:69], v41 offset:4096\n\t"
    "v_mfma_f32_16x16x32_bf16 v[72:75], a[124:127], v[188:191], v[72:75]\n\t"
    "buffer_load_dwordx4 v17, s[12:15], s65 offen offset:2048 lds\n\t"
    "ds_read_b64_tr_b16 a[70:71], v41 offset:5120\n\t"
    "s_waitcnt vmcnt(11)\n\t"
    "v_mfma_f32_16x16x32_bf16 v[60:63], a[128:131], v[192:195], v[60:63]\n\t"
    "ds_read_b64_tr_b16 a[72:73], v42 offset:4096\n\t"
    "v_mfma_f32_16x16x32_bf16 v[64:67], a[132:135], v[192:195], v[64:67]\n\t"
    "ds_read_b64_tr_b16 a[74:75], v42 offset:5120\n\t"
    "v_mfma_f32_16x16x32_bf16 v[68:71], a[136:139], v[192:195], v[68:71]\n\t"
    "ds_read_b64_tr_b16 a[76:77], v43 offset:4096\n\t"
    "v_mfma_f32_16x16x32_bf16 v[72:75], a[140:143], v[192:195], v[72:75]\n\t"
    "buffer_load_dwordx4 v18, s[12:15], s65 offen offset:3072 lds\n\t"
    "ds_read_b64_tr_b16 a[78:79], v43 offset:5120\n\t"
    "s_waitcnt vmcnt(10)\n\t"
    "v_mfma_f32_16x16x32_bf16 v[60:63], a[144:147], v[196:199], v[60:63]\n\t"
    "v_cmp_ne_u64 vcc, s[80:81], v[26:27]\n\t"
    "v_mfma_f32_16x16x32_bf16 v[64:67], a[148:151], v[196:199], v[64:67]\n\t"
    "s_cbranch_vccz L_bok%=\n\t"
    "s_mov_b64 exec, 0xffffffff\n\t"
    "L_bspin%=:\n\t"
    "v_mov_b32 v26, 0\n\t"
    "v_mov_b32 v27, 0\n\t"
    "buffer_atomic_add_x2 v[26:27], v21, s[16:19], 0 offen offset:256 sc0\n\t"
    "s_waitcnt vmcnt(0)\n\t"
    "v_cmp_ne_u64 vcc, s[80:81], v[26:27]\n\t"
    "s_sub_u32 s92, s92, 1\n\t"
    "s_cbranch_scc1 L_bto%=\n\t"
    "s_nop 2\n\t"
    "s_cbranch_vccnz L_bspin%=\n\t"
    "L_bto%=:\n\t"
    "s_mov_b64 vcc, 0\n\t"
    "s_mov_b64 exec, -1\n\t"
    "s_nop 3\n\t"
    "L_bok%=:\n\t"
    "buffer_load_dwordx4 v[224:227], v22, s[24:27], s70 offen offset:0 sc1\n\t"
    "ds_read_b64_tr_b16 a[80:81], v40 offset:8192\n\t"
    "ds_read_b64_tr_b16 a[82:83], v40 offset:9216\n\t"
    "v_mfma_f32_16x16x32_bf16 v[68:71], a[152:155], v[196:199], v[68:71]\n\t"
    "ds_read_b64_tr_b16 a[84:85], v41 offset:8192\n\t"
    "v_mfma_f32_16x16x32_bf16 v[72:75], a[156:159], v[196:199], v[72:75]\n\t"
    "buffer_load_dwordx4 v[228:231], v22, s[24:27], s70 offen offset:1024 sc1\n\t"
    "ds_read_b64_tr_b16 a[86:87], v41 offset:9216\n\t"
    "ds_read_b64_tr_b16 a[88:89], v42 offset:8192\n\t"
    "s_waitcnt vmcnt(11) lgkmcnt(15)\n\t"
    "v_mfma_f32_16x16x32_bf16 v[60:63], a[160:163], v[200:203], v[60:63]\n\t"
    "ds_read_b64_tr_b16 a[90:91], v42 offset:9216\n\t"
    "v_mfma_f32_16x16x32_bf16 v[64:67], a[164:167], v[200:203], v[64:67]\n\t"
    "buffer_load_dwordx4 v[232:235], v22, s[24:27], s70 offen offset:2048 sc1\n\t"
    "ds_read_b64_tr_b16 a[92:93], v43 offset:8192\n\t"
    "v_mfma_f32_16x16x32_bf16 v[68:71], a[168:171], v[200:203], v[68:71]\n\t"
    "ds_read_b64_tr_b16 a[94:95], v43 offset:9216\n\t"
    "v_mfma_f32_16x16x32_bf16 v[72:75], a[172:175], v[200:203], v[72:75]\n\t"
    "buffer_load_dwordx4 v[236:239], v22, s[24:27], s70 offen offset:3072 sc1\n\t"
    "ds_read_b64_tr_b16 a[96:97], v40 offset:12288\n\t"
    "s_waitcnt vmcnt(12)\n\t"
    "v_mfma_f32_16x16x32_bf16 v[60:63], a[176:179], v[204:207], v[60:63]\n\t"
    "ds_read_b64_tr_b16 a[98:99], v40 offset:13312\n\t"
    "v_mfma_f32_16x16x32_bf16 v[64:67], a[180:183], v[204:207], v[64:67]\n\t"
    "buffer_load_dwordx4 v[240:243], v22, s[24:27], s71 offen offset:0 sc1\n\t"
    "ds_read_b64_tr_b16 a[100:101], v41 offset:12288\n\t"
    "v_mfma_f32_16x16x32_bf16 v[68:71], a[184:187], v[204:207], v[68:71]\n\t"
    "ds_read_b64_tr_b16 a[102:103], v41 offset:13312\n\t"
    "v_mfma_f32_16x16x32_bf16 v[72:75], a[188:191], v[204:207], v[72:75]\n\t"
    "buffer_load_dwordx4 v[244:247], v22, s[24:27], s71 offen offset:1024 sc1\n\t"
    "ds_read_b64_tr_b16 a[104:105], v42 offset:12288\n\t"
    "s_waitcnt vmcnt(13)\n\t"
    "v_mfma_f32_16x16x32_bf16 v[60:63], a[192:195], v[208:211], v[60:63]\n\t"
    "ds_read_b64_tr_b16 a[106:107], v42 offset:13312\n\t"
    "v_mfma_f32_16x16x32_bf16 v[64:67], a[196:199], v[208:211], v[64:67]\n\t"
    "ds_read_b64_tr_b16 a[108:109], v43 offset:12288\n\t"
    "ds_read_b64_tr_b16 a[110:111], v43 offset:13312\n\t"
    "v_mfma_f32_16x16x32_bf16 v[68:71], a[200:203], v[208:211], v[68:71]\n\t"
    "ds_read_b64_tr_b16 a[112:113], v40 offset:16384\n\t"
    "v_mfma_f32_16x16x32_bf16 v[72:75], a[204:207], v[208:211], v[72:75]\n\t"
    "buffer_load_dwordx4 v[248:251], v22, s[24:27], s71 offen offset:2048 sc1\n\t"
    "ds_read_b64_tr_b16 a[114:115], v40 offset:17408\n\t"
    "s_waitcnt vmcnt(13)\n\t"
    "v_mfma_f32_16x16x32_bf16 v[60:63], a[208:211], v[212:215], v[60:63]\n\t"
    "ds_read_b64_tr_b16 a[116:117], v41 offset:16384\n\t"
    "v_mfma_f32_16x16x32_bf16 v[64:67], a[212:215], v[212:215], v[64:67]\n\t"
    "ds_read_b64_tr_b16 a[118:119], v41 offset:17408\n\t"
    "v_mfma_f32_16x16x32_bf16 v[68:71], a[216:219], v[212:215], v[68:71]\n\t"
    "ds_read_b64_tr_b16 a[120:121], v42 offset:16384\n\t"
    "v_mfma_f32_16x16x32_bf16 v[72:75], a[220:223], v[212:215], v[72:75]\n\t"
    "buffer_load_dwordx4 v[252:255], v22, s[24:27], s71 offen offset:3072 sc1\n\t"
    "ds_read_b64_tr_b16 a[122:123], v42 offset:17408\n\t"
    "s_waitcnt vmcnt(13)\n\t"
    "v_mfma_f32_16x16x32_bf16 v[60:63], a[224:227], v[216:219], v[60:63]\n\t"
    "ds_read_b64_tr_b16 a[124:125], v43 offset:16384\n\t"
    "v_mfma_f32_16x16x32_bf16 v[64:67], a[228:231], v[216:219], v[64:67]\n\t"
    "ds_read_b64_tr_b16 a[126:127], v43 offset:17408\n\t"
    "v_mfma_f32_16x16x32_bf16 v[68:71], a[232:235], v[216:219], v[68:71]\n\t"
    "ds_read_b64_tr_b16 a[128:129], v40 offset:20480\n\t"
    "v_mfma_f32_16x16x32_bf16 v[72:75], a[236:239], v[216:219], v[72:75]\n\t"
    "buffer_load_dwordx4 v[160:163], v22, s[24:27], s72 offen offset:0 sc1\n\t"
    "ds_read_b64_tr_b16 a[130:131], v40 offset:21504\n\t"
    "ds_read_b64_tr_b16 a[132:133], v41 offset:20480\n\t"
    "s_waitcnt vmcnt(13)\n\t"
    "v_mfma_f32_16x16x32_bf16 v[60:63], a[240:243], v[220:223], v[60:63]\n\t"
    "ds_read_b64_tr_b16 a[134:135], v41 offset:21504\n\t"
    "v_mfma_f32_16x16x32_bf16 v[64:67], a[244:247], v[220:223], v[64:67]\n\t"
    "ds_read_b64_tr_b16 a[136:137], v42 offset:20480\n\t"
    "v_mfma_f32_16x16x32_bf16 v[68:71], a[248:251], v[220:223], v[68:71]\n\t"
    "ds_read_b64_tr_b16 a[138:139], v42 offset:21504\n\t"
    "v_mfma_f32_16x16x32_bf16 v[72:75], a[252:255], v[220:223], v[72:75]\n\t"
    "buffer_load_dwordx4 v[164:167], v22, s[24:27], s72 offen offset:1024 sc1\n\t"
    "ds_read_b64_tr_b16 a[140:141], v43 offset:20480\n\t"
    "s_waitcnt vmcnt(9)\n\t"
    "v_mfma_f32_16x16x32_bf16 v[60:63], v[80:83], v[224:227], v[60:63]\n\t"
    "s_barrier\n\t"
    "ds_read_b64_tr_b16 a[142:143], v43 offset:21504\n\t"
    "v_mfma_f32_16x16x32_bf16 v[64:67], v[84:87], v[224:227], v[64:67]\n\t"
    "ds_read_b64_tr_b16 a[144:145], v36 offset:16384\n\t"
    "v_mfma_f32_16x16x32_bf16 v[68:71], v[88:91], v[224:227], v[68:71]\n\t"
    "ds_read_b64_tr_b16 a[146:147], v36 offset:17408\n\t"
    "v_mfma_f32_16x16x32_bf16 v[72:75], v[92:95], v[224:227], v[72:75]\n\t"
    "buffer_load_dwordx4 v[168:171], v22, s[24:27], s72 offen offset:2048 sc1\n\t"
    "ds_read_b64_tr_b16 a[148:149], v37 offset:16384\n\t"
    "s_waitcnt vmcnt(9)\n\t"
    "v_mfma_f32_16x16x32_bf16 v[60:63], v[96:99], v[228:231], v[60:63]\n\t"
    "ds_read_b64_tr_b16 a[150:151], v37 offset:17408\n\t"
    "v_mfma_f32_16x16x32_bf16 v[64:67], v[100:103], v[228:231], v[64:67]\n\t"
    "ds_read_b64_tr_b16 a[152:153], v38 offset:16384\n\t"
    "ds_read_b64_tr_b16 a[154:155], v38 offset:17408\n\t"
    "v_mfma_f32_16x16x32_bf16 v[68:71], v[104:107], v[228:231], v[68:71]\n\t"
    "ds_read_b64_tr_b16 a[156:157], v39 offset:16384\n\t"
    "v_mfma_f32_16x16x32_bf16 v[72:75], v[108:111], v[228:231], v[72:75]\n\t"
    "buffer_load_dwordx4 v[172:175], v22, s[24:27], s72 offen offset:3072 sc1\n\t"
    "ds_read_b64_tr_b16 a[158:159], v39 offset:17408\n\t"
    "s_waitcnt vmcnt(9)\n\t"
    "v_mfma_f32_16x16x32_bf16 v[60:63], v[112:115], v[232:235], v[60:63]\n\t"
    "ds_read_b64_tr_b16 a[160:161], v36 offset:20480\n\t"
    "v_mfma_f32_16x16x32_bf16 v[64:67], v[116:119], v[232:235], v[64:67]\n\t"
    "ds_read_b64_tr_b16 a[162:163], v36 offset:21504\n\t"
    "v_mfma_f32_16x16x32_bf16 v[68:71], v[120:123], v[232:235], v[68:71]\n\t"
    "ds_read_b64_tr_b16 a[164:165], v37 offset:20480\n\t"
    "v_mfma_f32_16x16x32_bf16 v[72:75], v[124:127], v[232:235], v[72:75]\n\t"
    "buffer_load_dwordx4 v[176:179], v22, s[24:27], s73 offen offset:0 sc1\n\t"
    "ds_read_b64_tr_b16 a[166:167], v37 offset:21504\n\t"
    "s_waitcnt vmcnt(9)\n\t"
    "v_mfma_f32_16x16x32_bf16 v[60:63], v[128:131], v[236:239], v[60:63]\n\t"
    "ds_read_b64_tr_b16 a[168:169], v38 offset:20480\n\t"
    "v_mfma_f32_16x16x32_bf16 v[64:67], v[132:135], v[236:239], v[64:67]\n\t"
    "ds_read_b64_tr_b16 a[170:171], v38 offset:21504\n\t"
    "v_mfma_f32_16x16x32_bf16 v[68:71], v[136:139], v[236:239], v[68:71]\n\t"
    "ds_read_b64_tr_b16 a[172:173], v39 offset:20480\n\t"
    "v_mfma_f32_16x16x32_bf16 v[72:75], v[140:143], v[236:239], v[72:75]\n\t"
    "buffer_load_dwordx4 v[180:183], v22, s[24:27], s73 offen offset:1024 sc1\n\t"
    "ds_read_b64_tr_b16 a[174:175], v39 offset:21504\n\t"
    "ds_read_b64_tr_b16 a[176:177], v36 offset:24576\n\t"
    "s_waitcnt vmcnt(9)\n\t"
    "v_mfma_f32_16x16x32_bf16 v[60:63], v[144:147], v[240:243], v[60:63]\n\t"
    "ds_read_b64_tr_b16 a[178:179], v36 offset:25600\n\t"
    "v_mfma_f32_16x16x32_bf16 v[64:67], v[148:151], v[240:243], v[64:67]\n\t"
    "ds_read_b64_tr_b16 a[180:181], v37 offset:24576\n\t"
    "v_mfma_f32_16x16x32_bf16 v[68:71], v[152:155], v[240:243], v[68:71]\n\t"
    "ds_read_b64_tr_b16 a[182:183], v37 offset:25600\n\t"
    "v_mfma_f32_16x16x32_bf16 v[72:75], v[156:159], v[240:243], v[72:75]\n\t"
    "buffer_load_dwordx4 v[184:187], v22, s[24:27], s73 offen offset:2048 sc1\n\t"
    "ds_read_b64_tr_b16 a[184:185], v38 offset:24576\n\t"
    "s_waitcnt vmcnt(9) lgkmcnt(15)\n\t"
    "v_mfma_f32_16x16x32_bf16 v[60:63], a[0:3], v[244:247], v[60:63]\n\t"
    "ds_read_b64_tr_b16 a[186:187], v38 offset:25600\n\t"
    "v_mfma_f32_16x16x32_bf16 v[64:67], a[4:7], v[244:247], v[64:67]\n\t"
    "ds_read_b64_tr_b16 a[188:189], v39 offset:24576\n\t"
    "v_mfma_f32_16x16x32_bf16 v[68:71], a[8:11], v[244:247], v[68:71]\n\t"
    "ds_read_b64_tr_b16 a[190:191], v39 offset:25600\n\t"
    "v_mfma_f32_16x16x32_bf16 v[72:75], a[12:15], v[244:247], v[72:75]\n\t"
    "buffer_load_dwordx4 v[188:191], v22, s[24:27], s73 offen offset:3072 sc1\n\t"
    "ds_read_b64_tr_b16 a[192:193], v36 offset:28672\n\t"
    "s_waitcnt vmcnt(9) lgkmcnt(15)\n\t"
    "v_mfma_f32_16x16x32_bf16 v[60:63], a[16:19], v[248:251], v[60:63]\n\t"
    "ds_read_b64_tr_b16 a[194:195], v36 offset:29696\n\t"
    "v_mfma_f32_16x16x32_bf16 v[64:67], a[20:23], v[248:251], v[64:67]\n\t"
    "ds_read_b64_tr_b16 a[196:197], v37 offset:28672\n\t"
    "ds_read_b64_tr_b16 a[198:199], v37 offset:29696\n\t"
    "v_mfma_f32_16x16x32_bf16 v[68:71], a[24:27], v[248:251], v[68:71]\n\t"
    "ds_read_b64_tr_b16 a[200:201], v38 offset:28672\n\t"
    "v_mfma_f32_16x16x32_bf16 v[72:75], a[28:31], v[248:251], v[72:75]\n\t"
    "buffer_load_dwordx4 v[192:195], v22, s[24:27], s74 offen offset:0 sc1\n\t"
    "ds_read_b64_tr_b16 a[202:203], v38 offset:29696\n\t"
    "s_waitcnt vmcnt(9) lgkmcnt(15)\n\t"
    "v_mfma_f32_16x16x32_bf16 v[60:63], a[32:35], v[252:255], v[60:63]\n\t"
    "ds_read_b64_tr_b16 a[204:205], v39 offset:28672\n\t"
    "v_mfma_f32_16x16x32_bf16 v[64:67], a[36:39], v[252:255], v[64:67]\n\t"
    "ds_read_b64_tr_b16 a[206:207], v39 offset:29696\n\t"
    "v_mfma_f32_16x16x32_bf16 v[68:71], a[40:43], v[252:255], v[68:71]\n\t"
    "ds_read_b64_tr_b16 a[208:209], v40 offset:24576\n\t"
    "v_mfma_f32_16x16x32_bf16 v[72:75], a[44:47], v[252:255], v[72:75]\n\t"
    "buffer_load_dwordx4 v[196:199], v22, s[24:27], s74 offen offset:1024 sc1\n\t"
    "ds_read_b64_tr_b16 a[210:211], v40 offset:25600\n\t"
    "s_waitcnt vmcnt(9) lgkmcnt(15)\n\t"
    "v_mfma_f32_16x16x32_bf16 v[60:63], a[48:51], v[160:163], v[60:63]\n\t"
    "ds_read_b64_tr_b16 a[212:213], v41 offset:24576\n\t"
    "v_mfma_f32_16x16x32_bf16 v[64:67], a[52:55], v[160:163], v[64:67]\n\t"
    "ds_read_b64_tr_b16 a[214:215], v41 offset:25600\n\t"
    "v_mfma_f32_16x16x32_bf16 v[68:71], a[56:59], v[160:163], v[68:71]\n\t"
    "ds_read_b64_tr_b16 a[216:217], v42 offset:24576\n\t"
    "v_mfma_f32_16x16x32_bf16 v[72:75], a[60:63], v[160:163], v[72:75]\n\t"
    "buffer_load_dwordx4 v[200:203], v22, s[24:27], s74 offen offset:2048 sc1\n\t"
    "ds_read_b64_tr_b16 a[218:219], v42 offset:25600\n\t"
    "ds_read_b64_tr_b16 a[220:221], v43 offset:24576\n\t"
    "s_waitcnt vmcnt(9) lgkmcnt(15)\n\t"
    "v_mfma_f32_16x16x32_bf16 v[60:63], a[64:67], v[164:167], v[60:63]\n\t"
    "ds_read_b64_tr_b16 a[222:223], v43 offset:25600\n\t"
    "v_mfma_f32_16x16x32_bf16 v[64:67], a[68:71], v[164:167], v[64:67]\n\t"
    "ds_read_b64_tr_b16 a[224:225], v40 offset:28672\n\t"
    "v_mfma_f32_16x16x32_bf16 v[68:71], a[72:75], v[164:167], v[68:71]\n\t"
    "ds_read_b64_tr_b16 a[226:227], v40 offset:29696\n\t"
    "v_mfma_f32_16x16x32_bf16 v[72:75], a[76:79], v[164:167], v[72:75]\n\t"
    "buffer_load_dwordx4 v[204:207], v22, s[24:27], s74 offen offset:3072 sc1\n\t"
    "ds_read_b64_tr_b16 a[228:229], v41 offset:28672\n\t"
    "s_waitcnt vmcnt(9) lgkmcnt(15)\n\t"
    "v_mfma_f32_16x16x32_bf16 v[60:63], a[80:83], v[168:171], v[60:63]\n\t"
    "ds_read_b64_tr_b16 a[230:231], v41 offset:29696\n\t"
    "v_mfma_f32_16x16x32_bf16 v[64:67], a[84:87], v[168:171], v[64:67]\n\t"
    "ds_read_b64_tr_b16 a[232:233], v42 offset:28672\n\t"
    "v_mfma_f32_16x16x32_bf16 v[68:71], a[88:91], v[168:171], v[68:71]\n\t"
    "ds_read_b64_tr_b16 a[234:235], v42 offset:29696\n\t"
    "v_mfma_f32_16x16x32_bf16 v[72:75], a[92:95], v[168:171], v[72:75]\n\t"
    "buffer_load_dwordx4 v[208:211], v22, s[24:27], s75 offen offset:0 sc1\n\t"
    "ds_read_b64_tr_b16 a[236:237], v43 offset:28672\n\t"
    "s_waitcnt vmcnt(9) lgkmcnt(15)\n\t"
    "v_mfma_f32_16x16x32_bf16 v[60:63], a[96:99], v[172:175], v[60:63]\n\t"
    "ds_read_b64_tr_b16 a[238:239], v43 offset:29696\n\t"
    "v_mfma_f32_16x16x32_bf16 v[64:67], a[100:103], v[172:175], v[64:67]\n\t"
    "ds_read_b64_tr_b16 a[240:241], v40 offset:32768\n\t"
    "ds_read_b64_tr_b16 a[242:243], v40 offset:33792\n\t"
    "v_mfma_f32_16x16x32_bf16 v[68:71], a[104:107], v[172:175], v[68:71]\n\t"
    "ds_read_b64_tr_b16 a[244:245], v41 offset:32768\n\t"
    "v_mfma_f32_16x16x32_bf16 v[72:75], a[108:111], v[172:175], v[72:75]\n\t"
    "buffer_load_dwordx4 v[212:215], v22, s[24:27], s75 offen offset:1024 sc1\n\t"
    "ds_read_b64_tr_b16 a[246:247], v41 offset:33792\n\t"
    "s_waitcnt vmcnt(9) lgkmcnt(15)\n\t"
    "v_mfma_f32_16x16x32_bf16 v[60:63], a[112:115], v[176:179], v[60:63]\n\t"
    "ds_read_b64_tr_b16 a[248:249], v42 offset:32768\n\t"
    "v_mfma_f32_16x16x32_bf16 v[64:67], a[116:119], v[176:179], v[64:67]\n\t"
    "ds_read_b64_tr_b16 a[250:251], v42 offset:33792\n\t"
    "v_mfma_f32_16x16x32_bf16 v[68:71], a[120:123], v[176:179], v[68:71]\n\t"
    "ds_read_b64_tr_b16 a[252:253], v43 offset:32768\n\t"
    "v_mfma_f32_16x16x32_bf16 v[72:75], a[124:127], v[176:179], v[72:75]\n\t"
    "buffer_load_dwordx4 v[216:219], v22, s[24:27], s75 offen offset:2048 sc1\n\t"
    "ds_read_b64_tr_b16 a[254:255], v43 offset:33792\n\t"
    "s_waitcnt vmcnt(9) lgkmcnt(15)\n\t"
    "v_mfma_f32_16x16x32_bf16 v[60:63], a[128:131], v[180:183], v[60:63]\n\t"
    "ds_read_b64_tr_b16 v[80:81], v40 offset:36864\n\t"
    "v_mfma_f32_16x16x32_bf16 v[64:67], a[132:135], v[180:183], v[64:67]\n\t"
    "ds_read_b64_tr_b16 v[82:83], v40 offset:37888\n\t"
    "v_mfma_f32_16x16x32_bf16 v[68:71], a[136:139], v[180:183], v[68:71]\n\t"
    "ds_read_b64_tr_b16 v[84:85], v41 offset:36864\n\t"
    "v_mfma_f32_16x16x32_bf16 v[72:75], a[140:143], v[180:183], v[72:75]\n\t"
    "buffer_load_dwordx4 v[220:223], v22, s[24:27], s75 offen offset:3072 sc1\n\t"
    "ds_read_b64_tr_b16 v[86:87], v41 offset:37888\n\t"
    "ds_read_b64_tr_b16 v[88:89], v42 offset:36864\n\t"
    "s_waitcnt vmcnt(9) lgkmcnt(15)\n\t"
    "v_mfma_f32_16x16x32_bf16 v[60:63], a[144:147], v[184:187], v[60:63]\n\t"
    "ds_read_b64_tr_b16 v[90:91], v42 offset:37888\n\t"
    "v_mfma_f32_16x16x32_bf16 v[64:67], a[148:151], v[184:187], v[64:67]\n\t"
    "ds_read_b64_tr_b16 v[92:93], v43 offset:36864\n\t"
    "v_mfma_f32_16x16x32_bf16 v[68:71], a[152:155], v[184:187], v[68:71]\n\t"
    "ds_read_b64_tr_b16 v[94:95], v43 offset:37888\n\t"
    "v_mfma_f32_16x16x32_bf16 v[72:75], a[156:159], v[184:187], v[72:75]\n\t"
    "buffer_load_dwordx4 v[224:227], v22, s[24:27], s76 offen offset:0 sc1\n\t"
    "ds_read_b64_tr_b16 v[96:97], v40 offset:40960\n\t"
    "s_waitcnt vmcnt(9) lgkmcnt(15)\n\t"
    "v_mfma_f32_16x16x32_bf16 v[60:63], a[160:163], v[188:191], v[60:63]\n\t"
    "ds_read_b64_tr_b16 v[98:99], v40 offset:41984\n\t"
    "v_mfma_f32_16x16x32_bf16 v[64:67], a[164:167], v[188:191], v[64:67]\n\t"
    "ds_read_b64_tr_b16 v[100:101], v41 offset:40960\n\t"
    "v_mfma_f32_16x16x32_bf16 v[68:71], a[168:171], v[188:191], v[68:71]\n\t"
    "ds_read_b64_tr_b16 v[102:103], v41 offset:41984\n\t"
    "v_mfma_f32_16x16x32_bf16 v[72:75], a[172:175], v[188:191], v[72:75]\n\t"
    "buffer_load_dwordx4 v[228:231], v22, s[24:27], s76 offen offset:1024 sc1\n\t"
    "ds_read_b64_tr_b16 v[104:105], v42 offset:40960\n\t"
    "s_waitcnt vmcnt(9) lgkmcnt(15)\n\t"
    "v_mfma_f32_16x16x32_bf16 v[60:63], a[176:179], v[192:195], v[60:63]\n\t"
    "ds_read_b64_tr_b16 v[106:107], v42 offset:41984\n\t"
    "v_mfma_f32_16x16x32_bf16 v[64:67], a[180:183], v[192:195], v[64:67]\n\t"
    "ds_read_b64_tr_b16 v[108:109], v43 offset:40960\n\t"
    "ds_read_b64_tr_b16 v[110:111], v43 offset:41984\n\t"
    "v_mfma_f32_16x16x32_bf16 v[68:71], a[184:187], v[192:195], v[68:71]\n\t"
    "ds_read_b64_tr_b16 v[112:113], v40 offset:45056\n\t"
    "v_mfma_f32_16x16x32_bf16 v[72:75], a[188:191], v[192:195], v[72:75]\n\t"
    "buffer_load_dwordx4 v[232:235], v22, s[24:27], s76 offen offset:2048 sc1\n\t"
    "ds_read_b64_tr_b16 v[114:115], v40 offset:46080\n\t"
    "s_waitcnt vmcnt(9) lgkmcnt(15)\n\t"
    "v_mfma_f32_16x16x32_bf16 v[60:63], a[192:195], v[196:199], v[60:63]\n\t"
    "ds_read_b64_tr_b16 v[116:117], v41 offset:45056\n\t"
    "v_mfma_f32_16x16x32_bf16 v[64:67], a[196:199], v[196:199], v[64:67]\n\t"
    "ds_read_b64_tr_b16 v[118:119], v41 offset:46080\n\t"
    "v_mfma_f32_16x16x32_bf16 v[68:71], a[200:203], v[196:199], v[68:71]\n\t"
    "ds_read_b64_tr_b16 v[120:121], v42 offset:45056\n\t"
    "v_mfma_f32_16x16x32_bf16 v[72:75], a[204:207], v[196:199], v[72:75]\n\t"
    "ds_read_b64_tr_b16 v[122:123], v42 offset:46080\n\t"
    "s_waitcnt vmcnt(8) lgkmcnt(15)\n\t"
    "v_mfma_f32_16x16x32_bf16 v[60:63], a[208:211], v[200:203], v[60:63]\n\t"
    "ds_read_b64_tr_b16 v[124:125], v43 offset:45056\n\t"
    "v_mfma_f32_16x16x32_bf16 v[64:67], a[212:215], v[200:203], v[64:67]\n\t"
    "ds_read_b64_tr_b16 v[126:127], v43 offset:46080\n\t"
    "v_mfma_f32_16x16x32_bf16 v[68:71], a[216:219], v[200:203], v[68:71]\n\t"
    "ds_read_b64_tr_b16 v[128:129], v36 offset:49152\n\t"
    "v_mfma_f32_16x16x32_bf16 v[72:75], a[220:223], v[200:203], v[72:75]\n\t"
    "ds_read_b64_tr_b16 v[130:131], v36 offset:50176\n\t"
    "ds_read_b64_tr_b16 v[132:133], v37 offset:49152\n\t"
    "s_waitcnt vmcnt(7) lgkmcnt(15)\n\t"
    "v_mfma_f32_16x16x32_bf16 v[60:63], a[224:227], v[204:207], v[60:63]\n\t"
    "ds_read_b64_tr_b16 v[134:135], v37 offset:50176\n\t"
    "v_mfma_f32_16x16x32_bf16 v[64:67], a[228:231], v[204:207], v[64:67]\n\t"
    "ds_read_b64_tr_b16 v[136:137], v38 offset:49152\n\t"
    "v_mfma_f32_16x16x32_bf16 v[68:71], a[232:235], v[204:207], v[68:71]\n\t"
    "ds_read_b64_tr_b16 v[138:139], v38 offset:50176\n\t"
    "v_mfma_f32_16x16x32_bf16 v[72:75], a[236:239], v[204:207], v[72:75]\n\t"
    "ds_read_b64_tr_b16 v[140:141], v39 offset:49152\n\t"
    "s_waitcnt vmcnt(6) lgkmcnt(15)\n\t"
    "v_mfma_f32_16x16x32_bf16 v[60:63], a[240:243], v[208:211], v[60:63]\n\t"
    "ds_read_b64_tr_b16 v[142:143], v39 offset:50176\n\t"
    "v_mfma_f32_16x16x32_bf16 v[64:67], a[244:247], v[208:211], v[64:67]\n\t"
    "ds_read_b64_tr_b16 v[144:145], v36 offset:53248\n\t"
    "v_mfma_f32_16x16x32_bf16 v[68:71], a[248:251], v[208:211], v[68:71]\n\t"
    "ds_read_b64_tr_b16 v[146:147], v36 offset:54272\n\t"
    "v_mfma_f32_16x16x32_bf16 v[72:75], a[252:255], v[208:211], v[72:75]\n\t"
    "ds_read_b64_tr_b16 v[148:149], v37 offset:53248\n\t"
    "s_waitcnt vmcnt(5) lgkmcnt(15)\n\t"
    "v_mfma_f32_16x16x32_bf16 v[60:63], v[80:83], v[212:215], v[60:63]\n\t"
    "ds_read_b64_tr_b16 v[150:151], v37 offset:54272\n\t"
    "v_mfma_f32_16x16x32_bf16 v[64:67], v[84:87], v[212:215], v[64:67]\n\t"
    "ds_read_b64_tr_b16 v[152:153], v38 offset:53248\n\t"
    "ds_read_b64_tr_b16 v[154:155], v38 offset:54272\n\t"
    "v_mfma_f32_16x16x32_bf16 v[68:71], v[88:91], v[212:215], v[68:71]\n\t"
    "ds_read_b64_tr_b16 v[156:157], v39 offset:53248\n\t"
    "v_mfma_f32_16x16x32_bf16 v[72:75], v[92:95], v[212:215], v[72:75]\n\t"
    "ds_read_b64_tr_b16 v[158:159], v39 offset:54272\n\t"
    "s_waitcnt vmcnt(4) lgkmcnt(15)\n\t"
    "v_mfma_f32_16x16x32_bf16 v[60:63], v[96:99], v[216:219], v[60:63]\n\t"
    "ds_read_b64_tr_b16 a[0:1], v36 offset:57344\n\t"
    "v_mfma_f32_16x16x32_bf16 v[64:67], v[100:103], v[216:219], v[64:67]\n\t"
    "ds_read_b64_tr_b16 a[2:3], v36 offset:58368\n\t"
    "v_mfma_f32_16x16x32_bf16 v[68:71], v[104:107], v[216:219], v[68:71]\n\t"
    "ds_read_b64_tr_b16 a[4:5], v37 offset:57344\n\t"
    "v_mfma_f32_16x16x32_bf16 v[72:75], v[108:111], v[216:219], v[72:75]\n\t"
    "ds_read_b64_tr_b16 a[6:7], v37 offset:58368\n\t"
    "s_waitcnt vmcnt(3) lgkmcnt(15)\n\t"
    "v_mfma_f32_16x16x32_bf16 v[60:63], v[112:115], v[220:223], v[60:63]\n\t"
    "ds_read_b64_tr_b16 a[8:9], v38 offset:57344\n\t"
    "v_mfma_f32_16x16x32_bf16 v[64:67], v[116:119], v[220:223], v[64:67]\n\t"
    "ds_read_b64_tr_b16 a[10:11], v38 offset:58368\n\t"
    "v_mfma_f32_16x16x32_bf16 v[68:71], v[120:123], v[220:223], v[68:71]\n\t"
    "ds_read_b64_tr_b16 a[12:13], v39 offset:57344\n\t"
    "v_mfma_f32_16x16x32_bf16 v[72:75], v[124:127], v[220:223], v[72:75]\n\t"
    "ds_read_b64_tr_b16 a[14:15], v39 offset:58368\n\t"
    "s_waitcnt vmcnt(2) lgkmcnt(15)\n\t"
    "v_mfma_f32_16x16x32_bf16 v[60:63], v[128:131], v[224:227], v[60:63]\n\t"
    "v_mfma_f32_16x16x32_bf16 v[64:67], v[132:135], v[224:227], v[64:67]\n\t"
    "v_mfma_f32_16x16x32_bf16 v[68:71], v[136:139], v[224:227], v[68:71]\n\t"
    "v_mfma_f32_16x16x32_bf16 v[72:75], v[140:143], v[224:227], v[72:75]\n\t"
    "s_waitcnt vmcnt(1) lgkmcnt(8)\n\t"
    "v_mfma_f32_16x16x32_bf16 v[60:63], v[144:147], v[228:231], v[60:63]\n\t"
    "v_mfma_f32_16x16x32_bf16 v[64:67], v[148:151], v[228:231], v[64:67]\n\t"
    "v_mfma_f32_16x16x32_bf16 v[68:71], v[152:155], v[228:231], v[68:71]\n\t"
    "v_mfma_f32_16x16x32_bf16 v[72:75], v[156:159], v[228:231], v[72:75]\n\t"
    "s_waitcnt vmcnt(0) lgkmcnt(0)\n\t"
    "v_mfma_f32_16x16x32_bf16 v[60:63], a[0:3], v[232:235], v[60:63]\n\t"
    "v_mfma_f32_16x16x32_bf16 v[64:67], a[4:7], v[232:235], v[64:67]\n\t"
    "v_mfma_f32_16x16x32_bf16 v[68:71], a[8:11], v[232:235], v[68:71]\n\t"
    "v_mfma_f32_16x16x32_bf16 v[72:75], a[12:15], v[232:235], v[72:75]\n\t"
    "s_nop 4\n\t"
    "v_cvt_pk_bf16_f32 v80, v60, v61\n\t"
    "v_cvt_pk_bf16_f32 v81, v62, v63\n\t"
    "v_cvt_pk_bf16_f32 v82, v64, v65\n\t"
    "v_cvt_pk_bf16_f32 v83, v66, v67\n\t"
    "v_cvt_pk_bf16_f32 v84, v68, v69\n\t"
    "v_cvt_pk_bf16_f32 v85, v70, v71\n\t"
    "v_cvt_pk_bf16_f32 v86, v72, v73\n\t"
    "v_cvt_pk_bf16_f32 v87, v74, v75\n\t"
    "ds_write_b64 v48, v[80:81]\n\t"
    "ds_write_b64 v49, v[82:83]\n\t"
    "ds_write_b64 v50, v[84:85]\n\t"
    "ds_write_b64 v51, v[86:87]\n\t"
    "s_waitcnt lgkmcnt(0)\n\t"
    "ds_read_b128 v[92:95], v52\n\t"
    "ds_read_b128 v[96:99], v52 offset:1024\n\t"
    "s_waitcnt lgkmcnt(1)\n\t"
    "buffer_store_dwordx4 v[92:95], v53, s[20:23], s84 offen sc1\n\t"
    "s_waitcnt lgkmcnt(0)\n\t"
    "buffer_store_dwordx4 v[96:99], v53, s[20:23], s85 offen sc1\n\t"
    "s_cmp_eq_u32 s30, 0\n\t"
    "s_cbranch_scc0 L_end%=\n\t"
    "s_mov_b64 exec, 1\n\t"
    "buffer_store_dwordx2 v[24:25], v23, s[16:19], 0 offen\n\t"
    "s_mov_b64 exec, -1\n\t"
    "L_end%=:\n\t"
    :
    : "s"(__builtin_amdgcn_kernarg_segment_ptr()), "s"(blockIdx.x), "v"(threadIdx.x), "s"((unsigned)(size_t)lds)
    : "memory", "vcc", "scc", "s4", "s5", "s6", "s7", "s8", "s9", "s10", "s11", "s12", "s13", "s14", "s15", "s16", "s17", "s18", "s19", "s20", "s21", "s22", "s23", "s24", "s25", "s26", "s27", "s28", "s29", "s30", "s31", "s34", "s35", "s36", "s37", "s38", "s39", "s40", "s41", "s42", "s43", "s44", "s45", "s46", "s47", "s48", "s49", "s50", "s51", "s52", "s53", "s54", "s55", "s56", "s57", "s58", "s59", "s60", "s61", "s62", "s63", "s64", "s65", "s66", "s67", "s68", "s69", "s70", "s71", "s72", "s73", "s74", "s75", "s76", "s77", "s78", "s79", "s80", "s81", "s82", "s83", "s84", "s85", "s86", "s87", "s88", "s89", "s90", "s91", "s92", "s93", "s94", "s95", "s96", "s97", "s98", "s99", "v1", "v2", "v3", "v4", "v5", "v6", "v7", "v8", "v9", "v10", "v11", "v12", "v13", "v14", "v15", "v16", "v17", "v18", "v19", "v20", "v21", "v22", "v23", "v24", "v25", "v26", "v27", "v28", "v29", "v30", "v31", "v32", "v33", "v34", "v35", "v36", "v37", "v38", "v39", "v40", "v41", "v42", "v43", "v44", "v45", "v46", "v47", "v48", "v49", "v50", "v51", "v52", "v53", "v54", "v55", "v56", "v57", "v58", "v59", "v60", "v61", "v62", "v63", "v64", "v65", "v66", "v67", "v68", "v69", "v70", "v71", "v72", "v73", "v74", "v75", "v76", "v77", "v78", "v79", "v80", "v81", "v82", "v83", "v84", "v85", "v86", "v87", "v88", "v89", "v90", "v91", "v92", "v93", "v94", "v95", "v96", "v97", "v98", "v99", "v100", "v101", "v102", "v103", "v104", "v105", "v106", "v107", "v108", "v109", "v110", "v111", "v112", "v113", "v114", "v115", "v116", "v117", "v118", "v119", "v120", "v121", "v122", "v123", "v124", "v125", "v126", "v127", "v128", "v129", "v130", "v131", "v132", "v133", "v134", "v135", "v136", "v137", "v138", "v139", "v140", "v141", "v142", "v143", "v144", "v145", "v146", "v147", "v148", "v149", "v150", "v151", "v152", "v153", "v154", "v155", "v156", "v157", "v158", "v159", "v160", "v161", "v162", "v163", "v164", "v165", "v166", "v167", "v168", "v169", "v170", "v171", "v172", "v173", "v174", "v175", "v176", "v177", "v178", "v179", "v180", "v181", "v182", "v183", "v184", "v185", "v186", "v187", "v188", "v189", "v190", "v191", "v192", "v193", "v194", "v195", "v196", "v197", "v198", "v199", "v200", "v201", "v202", "v203", "v204", "v205", "v206", "v207", "v208", "v209", "v210", "v211", "v212", "v213", "v214", "v215", "v216", "v217", "v218", "v219", "v220", "v221", "v222", "v223", "v224", "v225", "v226", "v227", "v228", "v229", "v230", "v231", "v232", "v233", "v234", "v235", "v236", "v237", "v238", "v239", "v240", "v241", "v242", "v243", "v244", "v245", "v246", "v247", "v248", "v249", "v250", "v251", "v252", "v253", "v254", "v255", "a0", "a1", "a2", "a3", "a4", "a5", "a6", "a7", "a8", "a9", "a10", "a11", "a12", "a13", "a14", "a15", "a16", "a17", "a18", "a19", "a20", "a21", "a22", "a23", "a24", "a25", "a26", "a27", "a28", "a29", "a30", "a31", "a32", "a33", "a34", "a35", "a36", "a37", "a38", "a39", "a40", "a41", "a42", "a43", "a44", "a45", "a46", "a47", "a48", "a49", "a50", "a51", "a52", "a53", "a54", "a55", "a56", "a57", "a58", "a59", "a60", "a61", "a62", "a63", "a64", "a65", "a66", "a67", "a68", "a69", "a70", "a71", "a72", "a73", "a74", "a75", "a76", "a77", "a78", "a79", "a80", "a81", "a82", "a83", "a84", "a85", "a86", "a87", "a88", "a89", "a90", "a91", "a92", "a93", "a94", "a95", "a96", "a97", "a98", "a99", "a100", "a101", "a102", "a103", "a104", "a105", "a106", "a107", "a108", "a109", "a110", "a111", "a112", "a113", "a114", "a115", "a116", "a117", "a118", "a119", "a120", "a121", "a122", "a123", "a124", "a125", "a126", "a127", "a128", "a129", "a130", "a131", "a132", "a133", "a134", "a135", "a136", "a137", "a138", "a139", "a140", "a141", "a142", "a143", "a144", "a145", "a146", "a147", "a148", "a149", "a150", "a151", "a152", "a153", "a154", "a155", "a156", "a157", "a158", "a159", "a160", "a161", "a162", "a163", "a164", "a165", "a166", "a167", "a168", "a169", "a170", "a171", "a172", "a173", "a174", "a175", "a176", "a177", "a178", "a179", "a180", "a181", "a182", "a183", "a184", "a185", "a186", "a187", "a188", "a189", "a190", "a191", "a192", "a193", "a194", "a195", "a196", "a197", "a198", "a199", "a200", "a201", "a202", "a203", "a204", "a205", "a206", "a207", "a208", "a209", "a210", "a211", "a212", "a213", "a214", "a215", "a216", "a217", "a218", "a219", "a220", "a221", "a222", "a223", "a224", "a225", "a226", "a227", "a228", "a229", "a230", "a231", "a232", "a233", "a234", "a235", "a236", "a237", "a238", "a239", "a240", "a241", "a242", "a243", "a244", "a245", "a246", "a247", "a248", "a249", "a250", "a251", "a252", "a253", "a254", "a255");
#endif
}

extern "C" void kernel_launch(void* const* d_in, const int* in_sizes, int n_in, void* d_out, int out_size, void* d_ws, size_t ws_size, hipStream_t stream) {
  swiglu_fused<<<256, 256, 0, stream>>>(d_in[0], d_in[1], d_in[2], d_in[3], d_out, d_ws);
}
```
